```python
import torch

def _softmax(x, axis=-1):
    return torch.softmax(x, dim=axis)
def _gelu(x):
    return torch.nn.functional.gelu(x, approximate="tanh")
def _silu(x):
    return torch.nn.functional.silu(x)
def _sigmoid(x):
    return torch.sigmoid(x)
def _layernorm(x, axis=-1, eps=1e-5):
    m = x.mean(dim=axis, keepdim=True)
    v = x.var(dim=axis, keepdim=True, unbiased=False)
    return (x - m) / torch.sqrt(v + eps)
def _scatter_add(tgt, idx, src, axis=0):
    return tgt.clone().index_add_(axis, idx, src)
def _segment_sum(src, seg, n):
    out = torch.zeros((n,) + src.shape[1:], dtype=src.dtype)
    return out.index_add_(0, seg, src)
def _pad(x, pw):
    flat = tuple(v for pair in reversed(pw) for v in pair)
    return torch.nn.functional.pad(x, flat)

kernel_name = 'standalone__gqa_block_cached__near_ridge'

def setup_inputs():

    _w = (lambda a: (lambda t: t.to(torch.bfloat16) if t.is_floating_point() else t)(torch.as_tensor(a)))
    B, Sq, D = 16, 64, 512
    Hq, Hkv, Sk, Dh = 32, 32, 64, 128
    assert Hq % Hkv == 0, (Hq, Hkv)
    return {
        "x": _w((lambda *s: torch.randn(*s, dtype=torch.bfloat16) * 0.8)(B, Sq, D)),
        "k_cache": _w((lambda *s: torch.randn(*s, dtype=torch.bfloat16) * 0.8)(B, Hkv, Sk, Dh)),
        "v_cache": _w((lambda *s: torch.randn(*s, dtype=torch.bfloat16) * 0.05)(B, Hkv, Sk, Dh)),
        "wq": _w((lambda *s: torch.randn(*s, dtype=torch.bfloat16) * 0.8)(D, Hq * Dh) * (D ** -0.5)),
        "wo": _w((lambda *s: torch.randn(*s, dtype=torch.bfloat16) * 0.05)(Hq * Dh, D) * ((Hq * Dh) ** -0.5)),
    }

def reference(x, k_cache, v_cache, wq, wo):
    B, Sq, D = x.shape
    Hq, Dh = 32, 128
    Hkv = k_cache.shape[1]
    rep = Hq // Hkv
    q = (lambda x, *d: x.permute(*d))((x @ wq).reshape(B, Sq, Hq, Dh), 0, 2, 1, 3)
    k = (lambda x, n, axis: torch.repeat_interleave(x, n, dim=axis))(k_cache, rep, axis=1)
    v = (lambda x, n, axis: torch.repeat_interleave(x, n, dim=axis))(v_cache, rep, axis=1)
    scores = torch.einsum("bhqd,bhkd->bhqk", q, k) * (Dh ** -0.5)
    p = _softmax(scores, axis=-1)
    o = torch.einsum("bhqk,bhkd->bhqd", p, v)
    return (lambda x, *d: x.permute(*d))(o, 0, 2, 1, 3).reshape(B, Sq, Hq * Dh) @ wo
```

```cpp
#include <hip/hip_runtime.h>
#include <hip/hip_bf16.h>
typedef __hip_bfloat16 bf16;
__global__ void k_q(const bf16* x, const bf16* wq, bf16* q) {
  int n = blockIdx.x * 256 + threadIdx.x; int m = blockIdx.y;
  float acc = 0.f;
  for (int k = 0; k < 512; ++k) acc += __bfloat162float(x[m * 512 + k]) * __bfloat162float(wq[k * 4096 + n]);
  q[m * 4096 + n] = __float2bfloat16(acc);
}
__global__ void k_attn(const bf16* q, const bf16* kc, const bf16* vc, bf16* o) {
  int qr = blockIdx.x, h = blockIdx.y, b = blockIdx.z, t = threadIdx.x;
  __shared__ float p[64];
  const bf16* qp = q + (size_t)(b * 64 + qr) * 4096 + h * 128;
  const bf16* kp = kc + ((size_t)(b * 32 + h) * 64 + t) * 128;
  float s = 0.f;
  for (int d = 0; d < 128; ++d) s += __bfloat162float(qp[d]) * __bfloat162float(kp[d]);
  s *= 0.08838834764831845f;
  float m = s;
  for (int off = 32; off >= 1; off >>= 1) m = fmaxf(m, __shfl_xor(m, off));
  float e = __expf(s - m);
  float sum = e;
  for (int off = 32; off >= 1; off >>= 1) sum += __shfl_xor(sum, off);
  p[t] = __bfloat162float(__float2bfloat16(e / sum));
  __syncthreads();
  for (int d = t; d < 128; d += 64) {
    float acc = 0.f;
    for (int k = 0; k < 64; ++k) acc += p[k] * __bfloat162float(vc[((size_t)(b * 32 + h) * 64 + k) * 128 + d]);
    o[(size_t)(b * 64 + qr) * 4096 + h * 128 + d] = __float2bfloat16(acc);
  }
}
__global__ void k_o(const bf16* o, const bf16* wo, bf16* out) {
  int n = blockIdx.x * 256 + threadIdx.x; int m = blockIdx.y;
  float acc = 0.f;
  for (int k = 0; k < 4096; ++k) acc += __bfloat162float(o[m * 4096 + k]) * __bfloat162float(wo[k * 512 + n]);
  out[m * 512 + n] = __float2bfloat16(acc);
}
extern "C" void kernel_launch(void* const* d_in, const int* in_sizes, int n_in,
                              void* d_out, int out_size, void* d_ws, size_t ws_size,
                              hipStream_t stream) {
  const bf16* x = (const bf16*)d_in[0]; const bf16* kc = (const bf16*)d_in[1]; const bf16* vc = (const bf16*)d_in[2];
  const bf16* wq = (const bf16*)d_in[3]; const bf16* wo = (const bf16*)d_in[4];
  bf16* q = (bf16*)d_ws; bf16* o = q + 1024 * 4096;
  k_q<<<dim3(16, 1024), 256, 0, stream>>>(x, wq, q);
  k_attn<<<dim3(64, 32, 16), 64, 0, stream>>>(q, kc, vc, o);
  k_o<<<dim3(2, 1024), 256, 0, stream>>>(o, wo, (bf16*)d_out);
}
```

```cpp
#include <hip/hip_runtime.h>
#include <hip/hip_bf16.h>

typedef __bf16 bf16x4 __attribute__((ext_vector_type(4)));
typedef __bf16 bf16x8 __attribute__((ext_vector_type(8)));
typedef float f32x16 __attribute__((ext_vector_type(16)));
typedef unsigned u32x4 __attribute__((ext_vector_type(4)));
#define LDSP __attribute__((address_space(3)))
#define GASP __attribute__((address_space(1)))
#define DI __device__ __forceinline__
#define MFMA(a, b, c) __builtin_amdgcn_mfma_f32_32x32x16_bf16((a), (b), (c), 0, 0, 0)
#define BAR() do { asm volatile("" ::: "memory"); __builtin_amdgcn_s_barrier(); asm volatile("" ::: "memory"); } while (0)
#define WAITV(n) asm volatile("s_waitcnt vmcnt(" #n ")" ::: "memory")
#define WAITL0() asm volatile("s_waitcnt lgkmcnt(0)" ::: "memory")

constexpr int LDS_V = 0;
constexpr int LDS_X = 32768;
constexpr int LDS_W = 65536;
constexpr int LDS_TOTAL = 163840;

DI void dma16(const char* g, LDSP char* l) {
  __builtin_amdgcn_global_load_lds((const GASP unsigned*)g, (LDSP unsigned*)l, 16, 0, 0);
}
DI bf16x8 tr_frag(LDSP char* p, int off_lo, int off_hi) {
  bf16x4 a = __builtin_amdgcn_ds_read_tr16_b64_v4bf16((LDSP bf16x4*)(p + off_lo));
  bf16x4 b = __builtin_amdgcn_ds_read_tr16_b64_v4bf16((LDSP bf16x4*)(p + off_hi));
  return __builtin_shufflevector(a, b, 0, 1, 2, 3, 4, 5, 6, 7);
}
DI bf16x8 pack8(const f32x16& x, int s) {
  bf16x8 p;
#pragma unroll
  for (int j = 0; j < 8; ++j) p[j] = (__bf16)x[8 * s + j];
  return p;
}

struct FragSet { bf16x8 a[2][4]; bf16x8 b[2]; };

__global__ __launch_bounds__(512, 2) void fused_kernel(const char* __restrict__ x, const char* __restrict__ kc,
                                                       const char* __restrict__ vc, const char* __restrict__ wq,
                                                       const char* __restrict__ wo, char* __restrict__ part) {
  __shared__ __attribute__((aligned(16))) char smem_[LDS_TOTAL];
  LDSP char* smem = (LDSP char*)smem_;
  const int wid = __builtin_amdgcn_readfirstlane(threadIdx.x >> 6);
  const int lane = threadIdx.x & 63;
  const int bid = blockIdx.x;
  const int hp = (bid & 7) * 2 + ((bid >> 3) & 1);
  const int b = bid >> 4;

  if (wid >= 4) {
    const int lw = wid - 4;
    const int hi = lane >> 5;
    const char* wq_e = wq + (size_t)(8 * lw + hi) * 8192 + hp * 512 + (((lane & 31) ^ (hi << 2)) << 4);
    const char* wq_o = wq + (size_t)(8 * lw + 2 + hi) * 8192 + hp * 512 + (((lane & 31) ^ ((2 + hi) << 2)) << 4);
    const char* xg = x + (size_t)b * 65536 + (size_t)(8 * lw + (lane >> 3)) * 1024 + (((lane & 7) ^ (4 * (lw & 1) + (lane >> 4))) << 4);
    const int c16 = ((lane & 15) ^ ((lane >> 4) << 2)) << 4;
    const char* wog = wo + (size_t)(hp * 256 + 16 * lw + (lane >> 4)) * 1024 + c16;
    const char* vg = vc + ((size_t)b * 32 + hp * 2) * 16384 + (size_t)(32 * lw + (lane >> 4)) * 256 + c16;

    auto issue_wq = [&](int s, int slot) {
      LDSP char* d = smem + LDS_W + slot * 16384 + lw * 4096;
      const size_t go = (size_t)s * 32 * 8192;
      dma16(wq_e + go, d);
      dma16(wq_o + go, d + 1024);
      dma16(wq_e + go + 4 * 8192, d + 2048);
      dma16(wq_o + go + 4 * 8192, d + 3072);
    };
    auto issue_x = [&](int v) {
      const int u = v >> 1, hf = v & 1;
      dma16(xg + hf * 32768 + u * 128, smem + LDS_X + (u & 3) * 8192 + hf * 4096 + lw * 1024);
    };
    auto issue_wo = [&](int t, int slot) {
      LDSP char* d = smem + LDS_W + slot * 16384 + lw * 4096;
      const size_t go = (size_t)(t & 3) * 64 * 1024 + (t >> 2) * 256;
      dma16(wog + go, d);
      dma16(wog + go + 4096, d + 1024);
      dma16(wog + go + 8192, d + 2048);
      dma16(wog + go + 12288, d + 3072);
    };
    issue_x(0);
    issue_wq(0, 0); issue_x(1);
    issue_wq(1, 1); issue_x(2);
#pragma unroll
    for (int i = 0; i < 8; ++i) dma16(vg + i * 1024, smem + LDS_V + lw * 8192 + i * 1024);
#pragma unroll
    for (int s = 2; s < 6; ++s) { issue_wq(s, s); issue_x(s + 1); }
    WAITV(33); BAR();
    WAITV(28); BAR();
    int slot = 0;
#pragma unroll 1
    for (int i = 2; i <= 10; ++i) {
      issue_wq(i + 4, slot); issue_x(i + 5);
      slot = (slot == 5) ? 0 : slot + 1;
      WAITV(20); BAR();
    }
    issue_wq(15, slot); slot = (slot == 5) ? 0 : slot + 1;
    WAITV(16); BAR();
#pragma unroll 1
    for (int t = 0; t < 5; ++t) {
      issue_wo(t, slot); slot = (slot == 5) ? 0 : slot + 1;
      WAITV(16); BAR();
    }
    issue_wo(5, slot); slot = (slot == 5) ? 0 : slot + 1;
    WAITV(20); BAR();
    WAITV(16); BAR();
#pragma unroll 1
    for (int t = 6; t < 16; ++t) {
      issue_wo(t, slot); slot = (slot == 5) ? 0 : slot + 1;
      WAITV(16); BAR();
    }
    WAITV(12); BAR();
    WAITV(8); BAR();
    WAITV(4); BAR();
    WAITV(0); BAR();
    return;
  }

  const int h = wid >> 1, qh = wid & 1;
  const int r = lane & 31, hh = lane >> 5;
  const int blk = (lane >> 4) & 1, q4 = (lane >> 2) & 3, p4 = lane & 3;
  const int pp = ((p4 & 1) << 1) | (p4 >> 1);
  const int pir = (r & 19) | ((r & 4) << 1) | ((r & 8) >> 1);

  bf16x8 kf[2][8];
  {
    const char* kb = kc + ((size_t)b * 32 + hp * 2 + h) * 16384 + pir * 256 + hh * 16;
#pragma unroll
    for (int kt = 0; kt < 2; ++kt)
#pragma unroll
      for (int s = 0; s < 8; ++s) kf[kt][s] = *(const bf16x8*)(kb + kt * 8192 + s * 32);
  }

  int woff[4];
#pragma unroll
  for (int dt = 0; dt < 4; ++dt) woff[dt] = LDS_W + (8 * hh + q4) * 512 + h * 256 + ((dt ^ q4) << 6) + blk * 32 + pp * 8;
  const int xrow = 32 * qh + r;
  int xoff[4];
#pragma unroll
  for (int ks = 0; ks < 4; ++ks) xoff[ks] = LDS_X + xrow * 128 + (((2 * ks + hh) ^ ((xrow >> 1) & 7)) << 4);

  f32x16 accq[4];
#pragma unroll
  for (int dt = 0; dt < 4; ++dt)
#pragma unroll
    for (int i = 0; i < 16; ++i) accq[dt][i] = 0.f;

  auto read_stage = [&](FragSet& f, int slot, int xu, int par) {
    LDSP char* wb = smem + slot * 16384;
    LDSP char* xb = smem + xu * 8192;
#pragma unroll
    for (int ks = 0; ks < 2; ++ks) {
      f.b[ks] = *(LDSP bf16x8*)(xb + xoff[2 * par + ks]);
#pragma unroll
      for (int dt = 0; dt < 4; ++dt) f.a[ks][dt] = tr_frag(wb + woff[dt], ks * 8192, ks * 8192 + 2048);
    }
  };
  auto mma_stage = [&](const FragSet& f) {
#pragma unroll
    for (int ks = 0; ks < 2; ++ks)
#pragma unroll
      for (int dt = 0; dt < 4; ++dt) accq[dt] = MFMA(f.a[ks][dt], f.b[ks], accq[dt]);
  };

  FragSet f0, f1;
  int slot = 0;
  BAR();
  read_stage(f0, 0, 0, 0);
  WAITL0();
  slot = 1;
#pragma unroll 1
  for (int u = 0; u < 8; ++u) {
    BAR();
    read_stage(f1, slot, u & 3, 1);
    slot = (slot == 5) ? 0 : slot + 1;
    mma_stage(f0);
    WAITL0();
    BAR();
    if (u < 7) {
      read_stage(f0, slot, (u + 1) & 3, 0);
      slot = (slot == 5) ? 0 : slot + 1;
    }
    mma_stage(f1);
    WAITL0();
  }

  bf16x8 qf[8];
#pragma unroll
  for (int dt = 0; dt < 4; ++dt) { qf[2 * dt] = pack8(accq[dt], 0); qf[2 * dt + 1] = pack8(accq[dt], 1); }
  f32x16 accs[2];
#pragma unroll
  for (int kt = 0; kt < 2; ++kt) {
#pragma unroll
    for (int i = 0; i < 16; ++i) accs[kt][i] = 0.f;
#pragma unroll
    for (int s = 0; s < 8; ++s) accs[kt] = MFMA(kf[kt][s], qf[s], accs[kt]);
  }
  float mx = accs[0][0];
#pragma unroll
  for (int kt = 0; kt < 2; ++kt)
#pragma unroll
    for (int i = 0; i < 16; ++i) mx = fmaxf(mx, accs[kt][i]);
  {
    auto sw = __builtin_amdgcn_permlane32_swap(__float_as_uint(mx), __float_as_uint(mx), false, false);
    mx = fmaxf(__uint_as_float(sw[0]), __uint_as_float(sw[1]));
  }
  const float sc = 0.08838834764831845f * 1.4426950408889634f;
  const float mb = mx * sc;
  float sum = 0.f;
#pragma unroll
  for (int kt = 0; kt < 2; ++kt)
#pragma unroll
    for (int i = 0; i < 16; ++i) { float e = __builtin_amdgcn_exp2f(accs[kt][i] * sc - mb); accs[kt][i] = e; sum += e; }
  {
    auto sw = __builtin_amdgcn_permlane32_swap(__float_as_uint(sum), __float_as_uint(sum), false, false);
    sum = __uint_as_float(sw[0]) + __uint_as_float(sw[1]);
  }
  const float inv = __builtin_amdgcn_rcpf(sum);
  bf16x8 pf[4];
#pragma unroll
  for (int kt = 0; kt < 2; ++kt) {
#pragma unroll
    for (int i = 0; i < 16; ++i) accs[kt][i] *= inv;
    pf[2 * kt] = pack8(accs[kt], 0); pf[2 * kt + 1] = pack8(accs[kt], 1);
  }
  {
    LDSP char* ob = smem + LDS_X + (qh * 16 + h * 8) * 1024 + lane * 16;
#pragma unroll
    for (int dt = 0; dt < 4; ++dt) {
      LDSP char* vb = smem + LDS_V + h * 16384 + (8 * hh + q4) * 256 + ((dt ^ q4) << 6) + blk * 32 + pp * 8;
      f32x16 acco;
#pragma unroll
      for (int i = 0; i < 16; ++i) acco[i] = 0.f;
#pragma unroll
      for (int s = 0; s < 4; ++s) acco = MFMA(tr_frag(vb, s * 4096, s * 4096 + 1024), pf[s], acco);
      *(LDSP bf16x8*)(ob + (2 * dt) * 1024) = pack8(acco, 0);
      *(LDSP bf16x8*)(ob + (2 * dt + 1) * 1024) = pack8(acco, 1);
    }
  }
  WAITL0();
  BAR();

  bf16x8 of[2][16];
#pragma unroll
  for (int qt = 0; qt < 2; ++qt)
#pragma unroll
    for (int ks = 0; ks < 16; ++ks) of[qt][ks] = *(LDSP bf16x8*)(smem + LDS_X + (qt * 16 + ks) * 1024 + lane * 16);
  const int wooff = LDS_W + (8 * hh + q4) * 256 + ((wid ^ q4) << 6) + blk * 32 + pp * 8;
  bf16x8 wa[4], wb2[4];
  auto read_wo = [&](bf16x8* f, int sl) {
    LDSP char* p = smem + sl * 16384 + wooff;
#pragma unroll
    for (int ks = 0; ks < 4; ++ks) f[ks] = tr_frag(p, ks * 4096, ks * 4096 + 1024);
  };
  read_wo(wa, slot); slot = (slot == 5) ? 0 : slot + 1;
  WAITL0();
  const auto prs = __builtin_amdgcn_make_buffer_rsrc(part, 0, 16 << 20, 0x00020000);
  const int pofs = ((b * 4 * 16 + 4 * wid + (r >> 4)) * 16 + hp) * 1024 + hh * 256 + (r & 15) * 16;
#pragma unroll 1
  for (int c = 0; c < 4; ++c) {
    f32x16 acc[2];
#pragma unroll
    for (int qt = 0; qt < 2; ++qt)
#pragma unroll
      for (int i = 0; i < 16; ++i) acc[qt][i] = 0.f;
#pragma unroll
    for (int tq = 0; tq < 4; ++tq) {
      bf16x8* cur = (tq & 1) ? wb2 : wa;
      bf16x8* nxt = (tq & 1) ? wa : wb2;
      if (!(tq == 3 && c == 3)) {
        BAR();
        read_wo(nxt, slot); slot = (slot == 5) ? 0 : slot + 1;
      }
#pragma unroll
      for (int ks = 0; ks < 4; ++ks)
#pragma unroll
        for (int qt = 0; qt < 2; ++qt) acc[qt] = MFMA(cur[ks], of[qt][4 * tq + ks], acc[qt]);
      WAITL0();
    }
#pragma unroll
    for (int qt = 0; qt < 2; ++qt)
#pragma unroll
      for (int a = 0; a < 2; ++a)
        __builtin_amdgcn_raw_buffer_store_b128(__builtin_bit_cast(u32x4, pack8(acc[qt], a)), prs,
                                               pofs + (c * 16 + 2 * qt) * 16384 + a * 512, 0, 16);
  }
}

__global__ __launch_bounds__(256) void reduce_kernel(const char* __restrict__ part, char* __restrict__ out) {
  const int bid = blockIdx.x, lane = threadIdx.x & 63, c = threadIdx.x >> 6;
  const int b = bid >> 4, j = bid & 15;
  const char* p = part + (size_t)((b * 4 + c) * 16 + j) * 16384 + lane * 16;
  u32x4 v[16];
#pragma unroll
  for (int i = 0; i < 16; ++i) v[i] = *(const u32x4*)(p + i * 1024);
  float s[8];
#pragma unroll
  for (int e = 0; e < 8; ++e) s[e] = 0.f;
#pragma unroll
  for (int i = 0; i < 16; ++i)
#pragma unroll
    for (int e = 0; e < 4; ++e) {
      s[2 * e] += __uint_as_float(v[i][e] << 16);
      s[2 * e + 1] += __uint_as_float(v[i][e] & 0xffff0000u);
    }
  bf16x8 o;
#pragma unroll
  for (int e = 0; e < 8; ++e) o[e] = (__bf16)s[e];
  const int w = j >> 2, qt = (j >> 1) & 1, hf = j & 1;
  const int row = b * 64 + 32 * qt + 16 * hf + (lane & 15);
  const int col = 128 * c + 32 * w + 16 * (lane >> 5) + 8 * ((lane >> 4) & 1);
  *(bf16x8*)(out + (size_t)row * 1024 + col * 2) = o;
}

extern "C" void kernel_launch(void* const* d_in, const int* in_sizes, int n_in, void* d_out, int out_size, void* d_ws,
                              size_t ws_size, hipStream_t stream) {
  fused_kernel<<<256, 512, 0, stream>>>((const char*)d_in[0], (const char*)d_in[1], (const char*)d_in[2],
                                        (const char*)d_in[3], (const char*)d_in[4], (char*)d_ws);
  reduce_kernel<<<256, 256, 0, stream>>>((const char*)d_ws, (char*)d_out);
}
```

```cpp
#include <hip/hip_runtime.h>
#include <hip/hip_bf16.h>

typedef __bf16 bf16x4 __attribute__((ext_vector_type(4)));
typedef __bf16 bf16x8 __attribute__((ext_vector_type(8)));
typedef float f32x16 __attribute__((ext_vector_type(16)));
typedef float f32x2 __attribute__((ext_vector_type(2)));
typedef unsigned u32x4 __attribute__((ext_vector_type(4)));
#define LDSP __attribute__((address_space(3)))
#define GASP __attribute__((address_space(1)))
#define DI __device__ __forceinline__
#define MFMA(a, b, c) __builtin_amdgcn_mfma_f32_32x32x16_bf16((a), (b), (c), 0, 0, 0)
#define BAR() do { asm volatile("" ::: "memory"); __builtin_amdgcn_s_barrier(); asm volatile("" ::: "memory"); } while (0)
#define WAITV(n) asm volatile("s_waitcnt vmcnt(" #n ")" ::: "memory")
#define SB0() __builtin_amdgcn_sched_barrier(0)
#define WAITL0() do { asm volatile("" ::: "memory"); SB0(); __builtin_amdgcn_s_waitcnt(0xC07F); SB0(); asm volatile("" ::: "memory"); } while (0)
#define PAUX 16
#define LPRIO 0
#define KVAUX 2

constexpr int LDS_X = 0;
constexpr int LDS_K = 32768;
constexpr int LDS_W = 65536;
constexpr int LDS_V = 131072;
constexpr int LDS_TOTAL = 163840;

template <int AUX>
DI void dma16(const char* g, LDSP char* l) {
  __builtin_amdgcn_global_load_lds((const GASP unsigned*)g, (LDSP unsigned*)l, 16, 0, AUX);
}
DI bf16x8 tr_frag(LDSP char* p, int off_lo, int off_hi) {
  bf16x4 a = __builtin_amdgcn_ds_read_tr16_b64_v4bf16((LDSP bf16x4*)(p + off_lo));
  bf16x4 b = __builtin_amdgcn_ds_read_tr16_b64_v4bf16((LDSP bf16x4*)(p + off_hi));
  return __builtin_shufflevector(a, b, 0, 1, 2, 3, 4, 5, 6, 7);
}
DI bf16x8 pack8(const f32x16& x, int s) {
  bf16x8 p;
#pragma unroll
  for (int j = 0; j < 8; ++j) p[j] = (__bf16)x[8 * s + j];
  return p;
}

#define TS()
#define TSREC()
#define TSDUMP()
#define CBAR() BAR()
#define CWAITL0() WAITL0()
#define STAMP(k)
struct FragSet { bf16x8 a[2][4]; bf16x8 b[2]; };

__global__ __launch_bounds__(512, 2) void fused_kernel(const char* __restrict__ x, const char* __restrict__ kc,
                                                       const char* __restrict__ vc, const char* __restrict__ wq,
                                                       const char* __restrict__ wo, char* __restrict__ part) {
  __shared__ __attribute__((aligned(16))) char smem_[LDS_TOTAL];
  LDSP char* smem = (LDSP char*)smem_;
  const int wid = __builtin_amdgcn_readfirstlane(threadIdx.x >> 6);
  const int lane = threadIdx.x & 63;
  const int bid = blockIdx.x;
  const int hp = (bid & 7) * 2 + ((bid >> 3) & 1);
  const int b = bid >> 4;
  const int u0 = ((b >> 1) + 4 * (hp & 1)) & 7;
  const int swp = b & 1;

  if (wid >= 4) {
    const int lw = wid - 4;
    const int hi = lane >> 5;
    const auto rq = __builtin_amdgcn_make_buffer_rsrc((void*)wq, 0, 4 << 20, 0x00020000);
    const auto ro = __builtin_amdgcn_make_buffer_rsrc((void*)wo, 0, 4 << 20, 0x00020000);
    const auto rx = __builtin_amdgcn_make_buffer_rsrc((void*)x, 0, 1 << 20, 0x00020000);
    const auto rk = __builtin_amdgcn_make_buffer_rsrc((void*)kc, 0, 8 << 20, 0x00020000);
    const auto rv = __builtin_amdgcn_make_buffer_rsrc((void*)vc, 0, 8 << 20, 0x00020000);
    const int wq_e = (8 * lw + hi) * 8192 + hp * 512 + (((lane & 31) ^ (hi << 2)) << 4);
    const int wq_o = (8 * lw + 2 + hi) * 8192 + hp * 512 + (((lane & 31) ^ ((2 + hi) << 2)) << 4);
    const int xg = b * 65536 + (8 * lw + (lane >> 3)) * 1024 + (((lane & 7) ^ (4 * (lw & 1) + (lane >> 4))) << 4);
    const int c16 = ((lane & 15) ^ ((lane >> 4) << 2)) << 4;
    const int wog = (hp * 256 + 16 * lw + (lane >> 4)) * 1024 + c16;
    const int kvb = (b * 32 + hp * 2) * 16384;
    const int vg = kvb + (16 * lw + (lane >> 4)) * 256 + c16;
    const int kg = kvb + (4 * lw + (lane >> 4)) * 256 + (((lane & 15) ^ (4 * lw + (lane >> 4))) << 4);
    const int wopf = (hp * 256 + b * 16 + lw * 4) * 1024 + lane * 64;
#define DMA(rs, vo, so, l, aux) __builtin_amdgcn_raw_ptr_buffer_load_lds(rs, (LDSP unsigned*)(l), 16, vo, so, 0, aux)
    __builtin_amdgcn_s_setprio(LPRIO);

    auto issue_wq = [&](int i) {
      const int ks = 2 * ((u0 + (i >> 1)) & 7) + ((i & 1) ^ swp);
      LDSP char* d = smem + LDS_W + (i & 3) * 16384 + lw * 4096;
      const int go = ks * 32 * 8192;
      DMA(rq, wq_e, go, d, 0);
      DMA(rq, wq_o, go, d + 1024, 0);
      DMA(rq, wq_e, go + 4 * 8192, d + 2048, 0);
      DMA(rq, wq_o, go + 4 * 8192, d + 3072, 0);
    };
    auto issue_x = [&](int u, int hf) {
      DMA(rx, xg, hf * 32768 + ((u0 + u) & 7) * 128, smem + LDS_X + (u & 3) * 8192 + hf * 4096 + lw * 1024, 0);
    };
    auto issue_wo = [&](int t) {
      LDSP char* d = smem + (t >= 8 ? t - 8 : t + 2) * 16384 + lw * 4096;
      const int go = (t & 3) * 64 * 1024 + (t >> 2) * 256;
      DMA(ro, wog, go, d, 0);
      DMA(ro, wog, go + 4096, d + 1024, 0);
      DMA(ro, wog, go + 8192, d + 2048, 0);
      DMA(ro, wog, go + 12288, d + 3072, 0);
    };
    auto issue_k = [&](int hd) {
#pragma unroll
      for (int i = 0; i < 4; ++i) DMA(rk, kg, hd * 16384 + i * 4096, smem + LDS_K + hd * 16384 + i * 4096 + lw * 1024, KVAUX);
    };
    auto issue_v = [&](int hd) {
#pragma unroll
      for (int i = 0; i < 4; ++i) DMA(rv, vg, hd * 16384 + i * 1024, smem + LDS_V + hd * 16384 + lw * 4096 + i * 1024, KVAUX);
    };
    issue_wq(0);
#pragma unroll
    for (int u = 0; u < 4; ++u) { issue_x(u, 0); issue_x(u, 1); }
    issue_k(0);
    WAITV(8); BAR();
    issue_wq(1); issue_k(1); issue_wq(2);
    WAITV(8); BAR();
    issue_v(0); issue_wq(3);
    WAITV(8); BAR();
    issue_v(1); issue_wq(4);
    WAITV(8); BAR();
    issue_wq(5); issue_x(4, 0);
#pragma unroll 1
    for (int i = 6; i <= 12; ++i) {
      issue_wq(i); issue_x((i + 3) >> 1, (i + 1) & 1);
      WAITV(10); BAR();
    }
    issue_wq(13); WAITV(9); BAR();
    issue_wq(14); WAITV(8); BAR();
    issue_wq(15);
    __builtin_amdgcn_raw_ptr_buffer_load_lds(ro, (LDSP unsigned*)(smem + LDS_K + lw * 4096), 4, wopf, 0, 0, 0);
    issue_wo(0);
    WAITV(13); BAR();
    WAITV(9); BAR();
    WAITV(5); BAR();
    issue_wo(1);
    BAR();
#pragma unroll 1
    for (int t = 2; t < 6; ++t) issue_wo(t);
    WAITV(0); BAR();
    issue_wo(6); issue_wo(7);
    BAR();
#pragma unroll 1
    for (int t = 8; t < 12; ++t) issue_wo(t);
    WAITV(8); BAR();
#pragma unroll 1
    for (int t = 12; t < 16; ++t) issue_wo(t);
    WAITV(8); BAR();
    WAITV(0); BAR();
    return;
  }

  const int h = wid >> 1, qh = wid & 1;
  const int r = lane & 31, hh = lane >> 5;
  const int blk = (lane >> 4) & 1, q4 = (lane >> 2) & 3, p4 = lane & 3;
  const int pp = ((p4 & 1) << 1) | (p4 >> 1);
  const int pir = (r & 19) | ((r & 4) << 1) | ((r & 8) >> 1);
  STAMP(0);

  int woff[4];
#pragma unroll
  for (int dt = 0; dt < 4; ++dt) woff[dt] = LDS_W + (8 * hh + q4) * 512 + h * 256 + ((dt ^ q4) << 6) + blk * 32 + pp * 8;
  const int xrow = 32 * qh + r;
  int xoff[4];
#pragma unroll
  for (int ks = 0; ks < 4; ++ks) xoff[ks] = LDS_X + xrow * 128 + (((2 * (ks ^ (2 * swp)) + hh) ^ ((xrow >> 1) & 7)) << 4);

  int voff[4];
#pragma unroll
  for (int dt = 0; dt < 4; ++dt) voff[dt] = LDS_V + h * 16384 + (8 * hh + q4) * 256 + ((dt ^ q4) << 6) + blk * 32 + pp * 8;
  int ooff = LDS_X + (qh * 16 + h * 8) * 1024 + lane * 16;
  asm volatile("" : "+v"(voff[0]), "+v"(voff[1]), "+v"(voff[2]), "+v"(voff[3]), "+v"(ooff));
  f32x16 accq[4];
#pragma unroll
  for (int dt = 0; dt < 4; ++dt)
#pragma unroll
    for (int i = 0; i < 16; ++i) accq[dt][i] = 0.f;

  auto read_stage = [&](FragSet& f, int slot, int xu, int par) {
    LDSP char* wb = smem + slot * 16384;
    LDSP char* xb = smem + xu * 8192;
#pragma unroll
    for (int ks = 0; ks < 2; ++ks) {
      f.b[ks] = *(LDSP bf16x8*)(xb + xoff[2 * par + ks]);
#pragma unroll
      for (int dt = 0; dt < 4; ++dt) f.a[ks][dt] = tr_frag(wb + woff[dt], ks * 8192, ks * 8192 + 2048);
    }
  };
  auto mma_stage = [&](const FragSet& f) {
#pragma unroll
    for (int ks = 0; ks < 2; ++ks)
#pragma unroll
      for (int dt = 0; dt < 4; ++dt) accq[dt] = MFMA(f.a[ks][dt], f.b[ks], accq[dt]);
  };
#define INTERLEAVE(NM, ND) do { _Pragma("unroll") for (int z = 0; z < NM; ++z) { \
    __builtin_amdgcn_sched_group_barrier(0x8, 1, 0); __builtin_amdgcn_sched_group_barrier(0x100, ND, 0); } } while (0)

  bf16x8 kf[2][8];
  FragSet f0, f1;
  CBAR();
  STAMP(1);
  read_stage(f0, 0, 0, 0);
  CWAITL0();
#pragma unroll 1
  for (int u = 0; u < 7; ++u) {
    CBAR();
    read_stage(f1, (2 * u + 1) & 3, u & 3, 1);
    mma_stage(f0);
    INTERLEAVE(8, 3);
    CWAITL0();
    CBAR();
    read_stage(f0, (2 * u + 2) & 3, (u + 1) & 3, 0);
    mma_stage(f1);
    INTERLEAVE(8, 3);
    CWAITL0();
    if (u == 4) {
      LDSP char* kb = smem + LDS_K + h * 16384 + pir * 256;
#pragma unroll
      for (int kt = 0; kt < 2; ++kt)
#pragma unroll
        for (int s = 0; s < 8; ++s) kf[kt][s] = *(LDSP bf16x8*)(kb + kt * 8192 + (((2 * s + hh) ^ (pir & 15)) << 4));
    }
  }
  CBAR();
  read_stage(f1, 3, 3, 1);
  mma_stage(f0);
  INTERLEAVE(8, 3);
  CWAITL0();
  CBAR();
  bf16x8 qf[8], vf[4][4], pf[4];
  f32x16 accs[2], acco[4];
  const f32x16 zero16 = {0.f, 0.f, 0.f, 0.f, 0.f, 0.f, 0.f, 0.f, 0.f, 0.f, 0.f, 0.f, 0.f, 0.f, 0.f, 0.f};
#define QM(dt, ks) accq[dt] = MFMA(f1.a[ks][dt], f1.b[ks], accq[dt])
#define QP(i) qf[i] = pack8(accq[(i) >> 1], (i) & 1)
#define SM(kt, s) accs[kt] = MFMA(kf[kt][s], qf[s], (s) == 0 ? zero16 : accs[kt])
#define VR(dt, s) vf[dt][s] = tr_frag(smem + voff[dt], (s) * 4096, (s) * 4096 + 1024)
  QM(0, 0); QM(0, 1); QM(1, 0); QM(1, 1); SB0();
  QP(0); SB0(); QM(2, 0); SB0();
  QP(1); SB0(); QM(2, 1); SB0();
  QP(2); SB0(); QM(3, 0); SB0();
  QP(3); SB0(); QM(3, 1); SB0();
  VR(0, 0); VR(1, 0); SB0(); SM(0, 0); SB0();
  QP(4); SB0(); SM(0, 1); SB0();
  QP(5); SB0(); SM(0, 2); SB0();
  QP(6); SB0(); SM(0, 3); SB0();
  QP(7); SB0(); SM(0, 4); SB0();
  VR(2, 0); VR(3, 0); SB0(); SM(0, 5); SB0();
  VR(0, 1); VR(1, 1); SB0(); SM(0, 6); SB0();
  VR(2, 1); VR(3, 1); SB0(); SM(0, 7); SB0();
  VR(0, 2); VR(0, 3); SB0(); SM(1, 0); SB0();
  VR(1, 2); VR(1, 3); SB0(); SM(1, 1); SB0();
  VR(2, 2); VR(2, 3); SB0(); SM(1, 2); SB0();
  VR(3, 2); VR(3, 3); SB0(); SM(1, 3); SB0();
  float mx = fmaxf(fmaxf(accs[0][0], accs[0][1]), accs[0][2]);
  mx = fmaxf(fmaxf(mx, accs[0][3]), accs[0][4]); mx = fmaxf(fmaxf(mx, accs[0][5]), accs[0][6]);
  SB0(); SM(1, 4); SB0();
  mx = fmaxf(fmaxf(mx, accs[0][7]), accs[0][8]); mx = fmaxf(fmaxf(mx, accs[0][9]), accs[0][10]); mx = fmaxf(fmaxf(mx, accs[0][11]), accs[0][12]);
  SB0(); SM(1, 5); SB0();
  mx = fmaxf(fmaxf(mx, accs[0][13]), accs[0][14]); mx = fmaxf(mx, accs[0][15]);
  SB0(); SM(1, 6); SM(1, 7); SB0();
  STAMP(2);
  {
    float m2 = fmaxf(fmaxf(accs[1][0], accs[1][1]), accs[1][2]);
#pragma unroll
    for (int i = 3; i < 9; i += 2) m2 = fmaxf(fmaxf(m2, accs[1][i]), accs[1][i + 1]);
#pragma unroll
    for (int i = 9; i < 15; i += 2) mx = fmaxf(fmaxf(mx, accs[1][i]), accs[1][i + 1]);
    mx = fmaxf(fmaxf(mx, m2), accs[1][15]);
    auto sw = __builtin_amdgcn_permlane32_swap(__float_as_uint(mx), __float_as_uint(mx), false, false);
    mx = fmaxf(__uint_as_float(sw[0]), __uint_as_float(sw[1]));
  }
  const float sc = 0.08838834764831845f * 1.4426950408889634f;
  const float mb = mx * sc;
  f32x2 sum2 = {0.f, 0.f}, sum3 = {0.f, 0.f};
#pragma unroll
  for (int kt = 0; kt < 2; ++kt)
#pragma unroll
    for (int i = 0; i < 16; i += 2) {
      f32x2 t = {accs[kt][i], accs[kt][i + 1]};
      t = t * sc - mb;
      t[0] = __builtin_amdgcn_exp2f(t[0]); t[1] = __builtin_amdgcn_exp2f(t[1]);
      accs[kt][i] = t[0]; accs[kt][i + 1] = t[1];
      if (i & 2) sum3 += t; else sum2 += t;
    }
  sum2 += sum3;
  float sum = sum2[0] + sum2[1];
  {
    auto sw = __builtin_amdgcn_permlane32_swap(__float_as_uint(sum), __float_as_uint(sum), false, false);
    sum = __uint_as_float(sw[0]) + __uint_as_float(sw[1]);
  }
  const float inv = __builtin_amdgcn_rcpf(sum);
  SB0();
#define PH(s, hf) do { _Pragma("unroll") for (int j = 4 * (hf); j < 4 * (hf) + 4; j += 2) { \
    f32x2 t = {accs[(s) >> 1][8 * ((s) & 1) + j], accs[(s) >> 1][8 * ((s) & 1) + j + 1]}; t *= inv; \
    pf[s][j] = (__bf16)t[0]; pf[s][j + 1] = (__bf16)t[1]; } } while (0)
#define OM(dt, s) acco[dt] = MFMA(vf[dt][s], pf[s], (s) == 0 ? zero16 : acco[dt])
#define OW(i) *(LDSP bf16x8*)(smem + ooff + (i) * 1024) = pack8(acco[(i) >> 1], (i) & 1)
  PH(0, 0); PH(0, 1); SB0();
  OM(0, 0); SB0(); PH(1, 0); SB0();
  OM(1, 0); SB0(); PH(1, 1); SB0();
  OM(2, 0); SB0(); PH(2, 0); SB0();
  OM(3, 0); SB0(); PH(2, 1); SB0();
  OM(0, 1); SB0(); PH(3, 0); SB0();
  OM(1, 1); SB0(); PH(3, 1); SB0();
  OM(2, 1); OM(3, 1); OM(0, 2); OM(0, 3); OM(1, 2); OM(1, 3); SB0();
  OW(0); SB0(); OM(2, 2); SB0();
  OW(1); SB0(); OM(2, 3); SB0();
  OW(2); SB0(); OM(3, 2); SB0();
  OW(3); SB0(); OM(3, 3); SB0();
  OW(4); OW(5); SB0(); OW(6); OW(7);
  CWAITL0();
  STAMP(3);
  CBAR();
  STAMP(4);

  int wob[3];
  wob[0] = (8 * hh + q4) * 256 + ((wid ^ q4) << 6) + blk * 32 + pp * 8; wob[1] = wob[0] + 65536; wob[2] = wob[0] + 131072;
  asm volatile("" : "+v"(wob[0]), "+v"(wob[1]), "+v"(wob[2]));
  bf16x8 wf[2][4];
  bf16x8 of[2][16];
  f32x16 acc[2][2];
  const auto prs = __builtin_amdgcn_make_buffer_rsrc(part, 0, 16 << 20, 0x00020000);
  const int pofs = ((b * 4 * 16 + 4 * wid + (r >> 4)) * 16 + hp) * 1024 + hh * 256 + (r & 15) * 16;
  auto read_wo = [&](int t) {
    const int sl = (t >= 8 ? t - 8 : t + 2);
    LDSP char* p = smem + wob[sl >> 2] + (sl & 3) * 16384;
#pragma unroll
    for (int ks = 0; ks < 4; ++ks) wf[t & 1][ks] = tr_frag(p, ks * 4096, ks * 4096 + 1024);
  };
  auto read_of = [&](int tq) {
#pragma unroll
    for (int ks = 4 * tq; ks < 4 * tq + 4; ++ks)
#pragma unroll
      for (int qt = 0; qt < 2; ++qt) of[qt][ks] = *(LDSP bf16x8*)(smem + LDS_X + (qt * 16 + ks) * 1024 + lane * 16);
  };
  auto store_chunk = [&](int c) {
#pragma unroll
    for (int qt = 0; qt < 2; ++qt)
#pragma unroll
      for (int a = 0; a < 2; ++a)
        __builtin_amdgcn_raw_buffer_store_b128(__builtin_bit_cast(u32x4, pack8(acc[c & 1][qt], a)), prs,
                                               pofs + (c * 16 + 2 * qt) * 16384 + a * 512, 0, PAUX);
  };
  read_wo(0); read_of(0);
  SB0();
#pragma unroll
  for (int t = 0; t < 16; ++t) {
    const int c = t >> 2, tq = t & 3;
    if (t == 2 || t == 5 || t == 9 || t == 13) { CWAITL0(); CBAR(); }
    if (t < 15) read_wo(t + 1);
    if (t == 0) { read_of(1); read_of(2); }
    if (t == 1) read_of(3);
    if (tq == 0) { acc[c & 1][0] = zero16; acc[c & 1][1] = zero16; }
#pragma unroll
    for (int ks = 0; ks < 4; ++ks)
#pragma unroll
      for (int qt = 0; qt < 2; ++qt) acc[c & 1][qt] = MFMA(wf[t & 1][ks], of[qt][4 * tq + ks], acc[c & 1][qt]);
    if (tq == 0 && c > 0) store_chunk(c - 1);
    if (t == 0) { INTERLEAVE(8, 3); }
    else if (t == 1) { INTERLEAVE(8, 2); }
    else if (tq == 0) {
      INTERLEAVE(4, 2);
#pragma unroll
      for (int z = 0; z < 4; ++z) { __builtin_amdgcn_sched_group_barrier(0x8, 1, 0); __builtin_amdgcn_sched_group_barrier(0x2, 5, 0); __builtin_amdgcn_sched_group_barrier(0x40, 1, 0); }
    } else { INTERLEAVE(4, 2); }
    SB0();
    if (tq == 3) STAMP(5 + c);
  }
  store_chunk(3);
  TS(); CWAITL0(); WAITV(0); TS(); CWAITL0(); TSDUMP();
}

__global__ __launch_bounds__(256) void reduce_kernel(char* __restrict__ part, char* __restrict__ out) {
  const int bid = blockIdx.x, lane = threadIdx.x & 63, c = threadIdx.x >> 6;
  const int b = bid >> 4, j = bid & 15;
  const char* p = part + (size_t)((b * 4 + c) * 16 + j) * 16384 + lane * 16;
  u32x4 v[16];
#pragma unroll
  for (int i = 0; i < 16; ++i) v[i] = *(const u32x4*)(p + i * 1024);
  float s[8];
#pragma unroll
  for (int e = 0; e < 8; ++e) s[e] = 0.f;
#pragma unroll
  for (int i = 0; i < 16; ++i)
#pragma unroll
    for (int e = 0; e < 4; ++e) {
      s[2 * e] += __uint_as_float(v[i][e] << 16);
      s[2 * e + 1] += __uint_as_float(v[i][e] & 0xffff0000u);
    }
  bf16x8 o;
#pragma unroll
  for (int e = 0; e < 8; ++e) o[e] = (__bf16)s[e];
  const int w = j >> 2, qt = (j >> 1) & 1, hf = j & 1;
  const int row = b * 64 + 32 * qt + 16 * hf + (lane & 15);
  const int col = 128 * c + 32 * w + 16 * (lane >> 5) + 8 * ((lane >> 4) & 1);
  *(bf16x8*)(out + (size_t)row * 1024 + col * 2) = o;
}

extern "C" void kernel_launch(void* const* d_in, const int* in_sizes, int n_in, void* d_out, int out_size, void* d_ws,
                              size_t ws_size, hipStream_t stream) {
  fused_kernel<<<256, 512, 0, stream>>>((const char*)d_in[0], (const char*)d_in[1], (const char*)d_in[2],
                                        (const char*)d_in[3], (const char*)d_in[4], (char*)d_ws);
  reduce_kernel<<<256, 256, 0, stream>>>((char*)d_ws, (char*)d_out);
}
```

```cpp
#define STAMPS
#define SEL 0
#include <hip/hip_runtime.h>
#include <hip/hip_bf16.h>

typedef __bf16 bf16x4 __attribute__((ext_vector_type(4)));
typedef __bf16 bf16x8 __attribute__((ext_vector_type(8)));
typedef float f32x16 __attribute__((ext_vector_type(16)));
typedef float f32x2 __attribute__((ext_vector_type(2)));
typedef unsigned u32x4 __attribute__((ext_vector_type(4)));
#define LDSP __attribute__((address_space(3)))
#define GASP __attribute__((address_space(1)))
#define DI __device__ __forceinline__
#define MFMA(a, b, c) __builtin_amdgcn_mfma_f32_32x32x16_bf16((a), (b), (c), 0, 0, 0)
#define BAR() do { asm volatile("" ::: "memory"); __builtin_amdgcn_s_barrier(); asm volatile("" ::: "memory"); } while (0)
#define WAITV(n) asm volatile("s_waitcnt vmcnt(" #n ")" ::: "memory")
#define SB0() __builtin_amdgcn_sched_barrier(0)
#define WAITL0() do { asm volatile("" ::: "memory"); SB0(); __builtin_amdgcn_s_waitcnt(0xC07F); SB0(); asm volatile("" ::: "memory"); } while (0)
#define PAUX 16
#define LPRIO 0
#define KVAUX 2

constexpr int LDS_X = 0;
constexpr int LDS_K = 32768;
constexpr int LDS_W = 65536;
constexpr int LDS_V = 131072;
constexpr int LDS_TOTAL = 163840;

template <int AUX>
DI void dma16(const char* g, LDSP char* l) {
  __builtin_amdgcn_global_load_lds((const GASP unsigned*)g, (LDSP unsigned*)l, 16, 0, AUX);
}
DI bf16x8 tr_frag(LDSP char* p, int off_lo, int off_hi) {
  bf16x4 a = __builtin_amdgcn_ds_read_tr16_b64_v4bf16((LDSP bf16x4*)(p + off_lo));
  bf16x4 b = __builtin_amdgcn_ds_read_tr16_b64_v4bf16((LDSP bf16x4*)(p + off_hi));
  return __builtin_shufflevector(a, b, 0, 1, 2, 3, 4, 5, 6, 7);
}
DI bf16x8 pack8(const f32x16& x, int s) {
  bf16x8 p;
#pragma unroll
  for (int j = 0; j < 8; ++j) p[j] = (__bf16)x[8 * s + j];
  return p;
}

#define TS()
#define TSREC()
#define TSDUMP()
#define CBAR() BAR()
#define CWAITL0() WAITL0()
#define STAMP_OFS (128u << 20)
#define NSTAMPS 16
#define STAMP(k) do { if (wid == 0 && lane == 0) ((unsigned long long*)(part + STAMP_OFS))[bid * 64 + (k)] = __builtin_amdgcn_s_memrealtime(); } while (0)
struct FragSet { bf16x8 a[2][4]; bf16x8 b[2]; };

__global__ __launch_bounds__(512, 2) void fused_kernel(const char* __restrict__ x, const char* __restrict__ kc,
                                                       const char* __restrict__ vc, const char* __restrict__ wq,
                                                       const char* __restrict__ wo, char* __restrict__ part) {
  __shared__ __attribute__((aligned(16))) char smem_[LDS_TOTAL];
  LDSP char* smem = (LDSP char*)smem_;
  const int wid = __builtin_amdgcn_readfirstlane(threadIdx.x >> 6);
  const int lane = threadIdx.x & 63;
  const int bid = blockIdx.x;
  const int hp = (bid & 7) * 2 + ((bid >> 3) & 1);
  const int b = bid >> 4;
  const int u0 = ((b >> 1) + 4 * (hp & 1)) & 7;
  const int swp = b & 1;

  if (wid >= 4) {
    const int lw = wid - 4;
    const int hi = lane >> 5;
    const auto rq = __builtin_amdgcn_make_buffer_rsrc((void*)wq, 0, 4 << 20, 0x00020000);
    const auto ro = __builtin_amdgcn_make_buffer_rsrc((void*)wo, 0, 4 << 20, 0x00020000);
    const auto rx = __builtin_amdgcn_make_buffer_rsrc((void*)x, 0, 1 << 20, 0x00020000);
    const auto rk = __builtin_amdgcn_make_buffer_rsrc((void*)kc, 0, 8 << 20, 0x00020000);
    const auto rv = __builtin_amdgcn_make_buffer_rsrc((void*)vc, 0, 8 << 20, 0x00020000);
    const int wq_e = (8 * lw + hi) * 8192 + hp * 512 + (((lane & 31) ^ (hi << 2)) << 4);
    const int wq_o = (8 * lw + 2 + hi) * 8192 + hp * 512 + (((lane & 31) ^ ((2 + hi) << 2)) << 4);
    const int xg = b * 65536 + (8 * lw + (lane >> 3)) * 1024 + (((lane & 7) ^ (4 * (lw & 1) + (lane >> 4))) << 4);
    const int c16 = ((lane & 15) ^ ((lane >> 4) << 2)) << 4;
    const int wog = (hp * 256 + 16 * lw + (lane >> 4)) * 1024 + c16;
    const int kvb = (b * 32 + hp * 2) * 16384;
    const int vg = kvb + (16 * lw + (lane >> 4)) * 256 + c16;
    const int kg = kvb + (4 * lw + (lane >> 4)) * 256 + (((lane & 15) ^ (4 * lw + (lane >> 4))) << 4);
    const int wopf = (hp * 256 + b * 16 + lw * 4) * 1024 + lane * 64;
#define DMA(rs, vo, so, l, aux) __builtin_amdgcn_raw_ptr_buffer_load_lds(rs, (LDSP unsigned*)(l), 16, vo, so, 0, aux)
    __builtin_amdgcn_s_setprio(LPRIO);

    auto issue_wq = [&](int i) {
      const int ks = 2 * ((u0 + (i >> 1)) & 7) + ((i & 1) ^ swp);
      LDSP char* d = smem + LDS_W + (i & 3) * 16384 + lw * 4096;
      const int go = ks * 32 * 8192;
      DMA(rq, wq_e, go, d, 0);
      DMA(rq, wq_o, go, d + 1024, 0);
      DMA(rq, wq_e, go + 4 * 8192, d + 2048, 0);
      DMA(rq, wq_o, go + 4 * 8192, d + 3072, 0);
    };
    auto issue_x = [&](int u, int hf) {
      DMA(rx, xg, hf * 32768 + ((u0 + u) & 7) * 128, smem + LDS_X + (u & 3) * 8192 + hf * 4096 + lw * 1024, 0);
    };
    auto issue_wo = [&](int t) {
      LDSP char* d = smem + (t >= 8 ? t - 8 : t + 2) * 16384 + lw * 4096;
      const int go = (t & 3) * 64 * 1024 + (t >> 2) * 256;
      DMA(ro, wog, go, d, 0);
      DMA(ro, wog, go + 4096, d + 1024, 0);
      DMA(ro, wog, go + 8192, d + 2048, 0);
      DMA(ro, wog, go + 12288, d + 3072, 0);
    };
    auto issue_k = [&](int hd) {
#pragma unroll
      for (int i = 0; i < 4; ++i) DMA(rk, kg, hd * 16384 + i * 4096, smem + LDS_K + hd * 16384 + i * 4096 + lw * 1024, KVAUX);
    };
    auto issue_v = [&](int hd) {
#pragma unroll
      for (int i = 0; i < 4; ++i) DMA(rv, vg, hd * 16384 + i * 1024, smem + LDS_V + hd * 16384 + lw * 4096 + i * 1024, KVAUX);
    };
    issue_wq(0);
#pragma unroll
    for (int u = 0; u < 4; ++u) { issue_x(u, 0); issue_x(u, 1); }
    issue_k(0);
    int lb[3];
    lb[0] = lw * 4096 + lane * 16; lb[1] = lb[0] + 65536; lb[2] = lb[0] + 131072;
    asm volatile("" : "+v"(lb[0]), "+v"(lb[1]), "+v"(lb[2]));
    WAITV(8); BAR();
    issue_wq(1); issue_k(1); issue_wq(2);
    WAITV(8); BAR();
    issue_v(0); issue_wq(3);
    WAITV(8); BAR();
    issue_v(1); issue_wq(4);
    WAITV(8); BAR();
    issue_wq(5); issue_x(4, 0);
#pragma unroll 1
    for (int i = 6; i <= 12; ++i) {
      issue_wq(i); issue_x((i + 3) >> 1, (i + 1) & 1);
      WAITV(10); BAR();
    }
    issue_wq(13); WAITV(9); BAR();
    issue_wq(14); WAITV(8); BAR();
    issue_wq(15);
    __builtin_amdgcn_raw_ptr_buffer_load_lds(ro, (LDSP unsigned*)(smem + LDS_K + lw * 4096), 4, wopf, 0, 0, 0);
    issue_wo(0);
    WAITV(13); BAR();
    WAITV(9); BAR();
    WAITV(5); BAR();
    issue_wo(1);
    BAR();
#pragma unroll 1
    for (int t = 2; t < 6; ++t) issue_wo(t);
    u32x4 st[10][4];
#define GET(t) do { _Pragma("unroll") for (int i = 0; i < 4; ++i) \
        asm volatile("buffer_load_dwordx4 %0, %1, %2, %3 offen" : "=v"(st[(t) - 6][i]) : "v"(wog), "s"(ro), "s"(((t) & 3) * 64 * 1024 + ((t) >> 2) * 256 + i * 4096) : "memory"); } while (0)
#define PUT(t, n) do { asm volatile("s_waitcnt vmcnt(" #n ")" : "+v"(st[(t) - 6][0]), "+v"(st[(t) - 6][1]), "+v"(st[(t) - 6][2]), "+v"(st[(t) - 6][3]) :: "memory"); \
      _Pragma("unroll") for (int i = 0; i < 4; ++i) { constexpr int sl = ((t) >= 8 ? (t) - 8 : (t) + 2); \
        *(LDSP u32x4*)(smem + lb[sl >> 2] + (sl & 3) * 16384 + i * 1024) = st[(t) - 6][i]; } } while (0)
    GET(6); GET(7); GET(8); GET(9); GET(10);
    WAITV(20); BAR();
    GET(11); GET(12); GET(13);
    PUT(6, 28); PUT(7, 24);
    WAITL0(); BAR();
    GET(14); GET(15);
    PUT(8, 28); PUT(9, 24); PUT(10, 20); PUT(11, 16);
    WAITL0(); BAR();
    PUT(12, 12); PUT(13, 8); PUT(14, 4); PUT(15, 0);
    WAITL0(); BAR();
    return;
  }

  const int h = wid >> 1, qh = wid & 1;
  const int r = lane & 31, hh = lane >> 5;
  const int blk = (lane >> 4) & 1, q4 = (lane >> 2) & 3, p4 = lane & 3;
  const int pp = ((p4 & 1) << 1) | (p4 >> 1);
  const int pir = (r & 19) | ((r & 4) << 1) | ((r & 8) >> 1);
  STAMP(0);

  int woff[4];
#pragma unroll
  for (int dt = 0; dt < 4; ++dt) woff[dt] = LDS_W + (8 * hh + q4) * 512 + h * 256 + ((dt ^ q4) << 6) + blk * 32 + pp * 8;
  const int xrow = 32 * qh + r;
  int xoff[4];
#pragma unroll
  for (int ks = 0; ks < 4; ++ks) xoff[ks] = LDS_X + xrow * 128 + (((2 * (ks ^ (2 * swp)) + hh) ^ ((xrow >> 1) & 7)) << 4);

  int voff[4];
#pragma unroll
  for (int dt = 0; dt < 4; ++dt) voff[dt] = LDS_V + h * 16384 + (8 * hh + q4) * 256 + ((dt ^ q4) << 6) + blk * 32 + pp * 8;
  int ooff = LDS_X + (qh * 16 + h * 8) * 1024 + lane * 16;
  asm volatile("" : "+v"(voff[0]), "+v"(voff[1]), "+v"(voff[2]), "+v"(voff[3]), "+v"(ooff));
  f32x16 accq[4];
#pragma unroll
  for (int dt = 0; dt < 4; ++dt)
#pragma unroll
    for (int i = 0; i < 16; ++i) accq[dt][i] = 0.f;

  auto read_stage = [&](FragSet& f, int slot, int xu, int par) {
    LDSP char* wb = smem + slot * 16384;
    LDSP char* xb = smem + xu * 8192;
#pragma unroll
    for (int ks = 0; ks < 2; ++ks) {
      f.b[ks] = *(LDSP bf16x8*)(xb + xoff[2 * par + ks]);
#pragma unroll
      for (int dt = 0; dt < 4; ++dt) f.a[ks][dt] = tr_frag(wb + woff[dt], ks * 8192, ks * 8192 + 2048);
    }
  };
  auto mma_stage = [&](const FragSet& f) {
#pragma unroll
    for (int ks = 0; ks < 2; ++ks)
#pragma unroll
      for (int dt = 0; dt < 4; ++dt) accq[dt] = MFMA(f.a[ks][dt], f.b[ks], accq[dt]);
  };
#define INTERLEAVE(NM, ND) do { _Pragma("unroll") for (int z = 0; z < NM; ++z) { \
    __builtin_amdgcn_sched_group_barrier(0x8, 1, 0); __builtin_amdgcn_sched_group_barrier(0x100, ND, 0); } } while (0)

  bf16x8 kf[2][8];
  FragSet f0, f1;
  CBAR();
  STAMP(1);
  read_stage(f0, 0, 0, 0);
  CWAITL0();
#pragma unroll 1
  for (int u = 0; u < 7; ++u) {
    CBAR();
    read_stage(f1, (2 * u + 1) & 3, u & 3, 1);
    mma_stage(f0);
    INTERLEAVE(8, 3);
    CWAITL0();
    CBAR();
    read_stage(f0, (2 * u + 2) & 3, (u + 1) & 3, 0);
    mma_stage(f1);
    INTERLEAVE(8, 3);
    CWAITL0();
    if (u == 4) {
      LDSP char* kb = smem + LDS_K + h * 16384 + pir * 256;
#pragma unroll
      for (int kt = 0; kt < 2; ++kt)
#pragma unroll
        for (int s = 0; s < 8; ++s) kf[kt][s] = *(LDSP bf16x8*)(kb + kt * 8192 + (((2 * s + hh) ^ (pir & 15)) << 4));
      STAMP(12);
    }
  }
  CBAR();
  read_stage(f1, 3, 3, 1);
  mma_stage(f0);
  INTERLEAVE(8, 3);
  CWAITL0();
  CBAR();
  bf16x8 qf[8], vf[4][4], pf[4];
  f32x16 accs[2], acco[4];
  const f32x16 zero16 = {0.f, 0.f, 0.f, 0.f, 0.f, 0.f, 0.f, 0.f, 0.f, 0.f, 0.f, 0.f, 0.f, 0.f, 0.f, 0.f};
#define QM(dt, ks) accq[dt] = MFMA(f1.a[ks][dt], f1.b[ks], accq[dt])
#define QP(i) qf[i] = pack8(accq[(i) >> 1], (i) & 1)
#define SM(kt, s) accs[kt] = MFMA(kf[kt][s], qf[s], (s) == 0 ? zero16 : accs[kt])
#define VR(dt, s) vf[dt][s] = tr_frag(smem + voff[dt], (s) * 4096, (s) * 4096 + 1024)
  QM(0, 0); QM(0, 1); QM(1, 0); QM(1, 1); SB0();
  QP(0); SB0(); QM(2, 0); SB0();
  QP(1); SB0(); QM(2, 1); SB0();
  QP(2); SB0(); QM(3, 0); SB0();
  QP(3); SB0(); QM(3, 1); SB0();
  VR(0, 0); VR(1, 0); SB0(); SM(0, 0); SB0();
  QP(4); SB0(); SM(0, 1); SB0();
  QP(5); SB0(); SM(0, 2); SB0();
  QP(6); SB0(); SM(0, 3); SB0();
  QP(7); SB0(); SM(0, 4); SB0();
  VR(2, 0); VR(3, 0); SB0(); SM(0, 5); SB0();
  VR(0, 1); VR(1, 1); SB0(); SM(0, 6); SB0();
  VR(2, 1); VR(3, 1); SB0(); SM(0, 7); SB0();
  VR(0, 2); VR(0, 3); SB0(); SM(1, 0); SB0();
  VR(1, 2); VR(1, 3); SB0(); SM(1, 1); SB0();
  VR(2, 2); VR(2, 3); SB0(); SM(1, 2); SB0();
  VR(3, 2); VR(3, 3); SB0(); SM(1, 3); SB0();
  float mx = fmaxf(fmaxf(accs[0][0], accs[0][1]), accs[0][2]);
  mx = fmaxf(fmaxf(mx, accs[0][3]), accs[0][4]); mx = fmaxf(fmaxf(mx, accs[0][5]), accs[0][6]);
  SB0(); SM(1, 4); SB0();
  mx = fmaxf(fmaxf(mx, accs[0][7]), accs[0][8]); mx = fmaxf(fmaxf(mx, accs[0][9]), accs[0][10]); mx = fmaxf(fmaxf(mx, accs[0][11]), accs[0][12]);
  SB0(); SM(1, 5); SB0();
  mx = fmaxf(fmaxf(mx, accs[0][13]), accs[0][14]); mx = fmaxf(mx, accs[0][15]);
  SB0(); SM(1, 6); SM(1, 7); SB0();
  STAMP(2);
  {
    float m2 = fmaxf(fmaxf(accs[1][0], accs[1][1]), accs[1][2]);
#pragma unroll
    for (int i = 3; i < 9; i += 2) m2 = fmaxf(fmaxf(m2, accs[1][i]), accs[1][i + 1]);
#pragma unroll
    for (int i = 9; i < 15; i += 2) mx = fmaxf(fmaxf(mx, accs[1][i]), accs[1][i + 1]);
    mx = fmaxf(fmaxf(mx, m2), accs[1][15]);
    auto sw = __builtin_amdgcn_permlane32_swap(__float_as_uint(mx), __float_as_uint(mx), false, false);
    mx = fmaxf(__uint_as_float(sw[0]), __uint_as_float(sw[1]));
  }
  const float sc = 0.08838834764831845f * 1.4426950408889634f;
  const float mb = mx * sc;
  f32x2 sum2 = {0.f, 0.f}, sum3 = {0.f, 0.f};
#pragma unroll
  for (int kt = 0; kt < 2; ++kt)
#pragma unroll
    for (int i = 0; i < 16; i += 2) {
      f32x2 t = {accs[kt][i], accs[kt][i + 1]};
      t = t * sc - mb;
      t[0] = __builtin_amdgcn_exp2f(t[0]); t[1] = __builtin_amdgcn_exp2f(t[1]);
      accs[kt][i] = t[0]; accs[kt][i + 1] = t[1];
      if (i & 2) sum3 += t; else sum2 += t;
    }
  sum2 += sum3;
  float sum = sum2[0] + sum2[1];
  {
    auto sw = __builtin_amdgcn_permlane32_swap(__float_as_uint(sum), __float_as_uint(sum), false, false);
    sum = __uint_as_float(sw[0]) + __uint_as_float(sw[1]);
  }
  const float inv = __builtin_amdgcn_rcpf(sum);
  SB0();
#define PH(s, hf) do { _Pragma("unroll") for (int j = 4 * (hf); j < 4 * (hf) + 4; j += 2) { \
    f32x2 t = {accs[(s) >> 1][8 * ((s) & 1) + j], accs[(s) >> 1][8 * ((s) & 1) + j + 1]}; t *= inv; \
    pf[s][j] = (__bf16)t[0]; pf[s][j + 1] = (__bf16)t[1]; } } while (0)
#define OM(dt, s) acco[dt] = MFMA(vf[dt][s], pf[s], (s) == 0 ? zero16 : acco[dt])
#define OW(i) *(LDSP bf16x8*)(smem + ooff + (i) * 1024) = pack8(acco[(i) >> 1], (i) & 1)
  PH(0, 0); PH(0, 1); SB0();
  OM(0, 0); SB0(); PH(1, 0); SB0();
  OM(1, 0); SB0(); PH(1, 1); SB0();
  OM(2, 0); SB0(); PH(2, 0); SB0();
  OM(3, 0); SB0(); PH(2, 1); SB0();
  OM(0, 1); SB0(); PH(3, 0); SB0();
  OM(1, 1); SB0(); PH(3, 1); SB0();
  OM(2, 1); OM(3, 1); OM(0, 2); OM(0, 3); OM(1, 2); OM(1, 3); SB0();
  OW(0); SB0(); OM(2, 2); SB0();
  OW(1); SB0(); OM(2, 3); SB0();
  OW(2); SB0(); OM(3, 2); SB0();
  OW(3); SB0(); OM(3, 3); SB0();
  OW(4); OW(5); SB0(); OW(6); OW(7);
  CWAITL0();
  STAMP(3);
  CBAR();
  STAMP(4);

  int wob[3];
  wob[0] = (8 * hh + q4) * 256 + ((wid ^ q4) << 6) + blk * 32 + pp * 8; wob[1] = wob[0] + 65536; wob[2] = wob[0] + 131072;
  asm volatile("" : "+v"(wob[0]), "+v"(wob[1]), "+v"(wob[2]));
  bf16x8 wf[2][4];
  bf16x8 of[2][16];
  f32x16 acc[2][2];
  const auto prs = __builtin_amdgcn_make_buffer_rsrc(part, 0, 16 << 20, 0x00020000);
  const int pofs = ((b * 4 * 16 + 4 * wid + (r >> 4)) * 16 + hp) * 1024 + hh * 256 + (r & 15) * 16;
  auto read_wo = [&](int t) {
    const int sl = (t >= 8 ? t - 8 : t + 2);
    LDSP char* p = smem + wob[sl >> 2] + (sl & 3) * 16384;
#pragma unroll
    for (int ks = 0; ks < 4; ++ks) wf[t & 1][ks] = tr_frag(p, ks * 4096, ks * 4096 + 1024);
  };
  auto read_of = [&](int tq) {
#pragma unroll
    for (int ks = 4 * tq; ks < 4 * tq + 4; ++ks)
#pragma unroll
      for (int qt = 0; qt < 2; ++qt) of[qt][ks] = *(LDSP bf16x8*)(smem + LDS_X + (qt * 16 + ks) * 1024 + lane * 16);
  };
  auto store_chunk = [&](int c) {
#pragma unroll
    for (int qt = 0; qt < 2; ++qt)
#pragma unroll
      for (int a = 0; a < 2; ++a)
        __builtin_amdgcn_raw_buffer_store_b128(__builtin_bit_cast(u32x4, pack8(acc[c & 1][qt], a)), prs,
                                               pofs + (c * 16 + 2 * qt) * 16384 + a * 512, 0, PAUX);
  };
  read_wo(0); read_of(0);
  SB0();
#pragma unroll
  for (int t = 0; t < 16; ++t) {
    const int c = t >> 2, tq = t & 3;
    if (t == 2 || t == 6 || t == 11) { CWAITL0(); CBAR(); }
    if (t < 15) read_wo(t + 1);
    if (t == 0) { read_of(1); read_of(2); }
    if (t == 1) read_of(3);
    if (tq == 0) { acc[c & 1][0] = zero16; acc[c & 1][1] = zero16; }
#pragma unroll
    for (int ks = 0; ks < 4; ++ks)
#pragma unroll
      for (int qt = 0; qt < 2; ++qt) acc[c & 1][qt] = MFMA(wf[t & 1][ks], of[qt][4 * tq + ks], acc[c & 1][qt]);
    if (tq == 0 && c > 0) store_chunk(c - 1);
    if (t == 0) { INTERLEAVE(8, 3); }
    else if (t == 1) { INTERLEAVE(8, 2); }
    else if (tq == 0) {
      INTERLEAVE(4, 2);
#pragma unroll
      for (int z = 0; z < 4; ++z) { __builtin_amdgcn_sched_group_barrier(0x8, 1, 0); __builtin_amdgcn_sched_group_barrier(0x2, 5, 0); __builtin_amdgcn_sched_group_barrier(0x40, 1, 0); }
    } else { INTERLEAVE(4, 2); }
    SB0();
    if (tq == 3) STAMP(5 + c);
  }
  store_chunk(3);
  WAITV(0);
  STAMP(9);
  TS(); CWAITL0(); WAITV(0); TS(); CWAITL0(); TSDUMP();
}

__global__ __launch_bounds__(256) void reduce_kernel(char* __restrict__ part, char* __restrict__ out) {
  const int bid = blockIdx.x, lane = threadIdx.x & 63, c = threadIdx.x >> 6;
  const int b = bid >> 4, j = bid & 15;
  const int wid = c;
  STAMP(10);
  const char* p = part + (size_t)((b * 4 + c) * 16 + j) * 16384 + lane * 16;
  u32x4 v[16];
#pragma unroll
  for (int i = 0; i < 16; ++i) v[i] = *(const u32x4*)(p + i * 1024);
  float s[8];
#pragma unroll
  for (int e = 0; e < 8; ++e) s[e] = 0.f;
#pragma unroll
  for (int i = 0; i < 16; ++i)
#pragma unroll
    for (int e = 0; e < 4; ++e) {
      s[2 * e] += __uint_as_float(v[i][e] << 16);
      s[2 * e + 1] += __uint_as_float(v[i][e] & 0xffff0000u);
    }
  bf16x8 o;
#pragma unroll
  for (int e = 0; e < 8; ++e) o[e] = (__bf16)s[e];
  const int w = j >> 2, qt = (j >> 1) & 1, hf = j & 1;
  const int row = b * 64 + 32 * qt + 16 * hf + (lane & 15);
  const int col = 128 * c + 32 * w + 16 * (lane >> 5) + 8 * ((lane >> 4) & 1);
  *(bf16x8*)(out + (size_t)row * 1024 + col * 2) = o;
  WAITV(0);
  STAMP(11);
}

__global__ void spin_k(const unsigned long long* st) {
  __shared__ unsigned long long sa[256], sb[256];
  int t = threadIdx.x;
  sa[t] = st[t * 64]; sb[t] = st[t * 64 + SEL]; __syncthreads();
  if (t == 0) {
    unsigned long long mn = ~0ull, mx = 0;
    for (int i = 0; i < 256; ++i) { mn = sa[i] < mn ? sa[i] : mn; mx = sb[i] > mx ? sb[i] : mx; }
    unsigned long long span = mx - mn; if (span > 10000) span = 10000;
    unsigned long long t0 = __builtin_amdgcn_s_memrealtime();
    while (__builtin_amdgcn_s_memrealtime() - t0 < span * 50) __builtin_amdgcn_s_sleep(1);
  }
}
extern "C" void kernel_launch(void* const* d_in, const int* in_sizes, int n_in, void* d_out, int out_size, void* d_ws,
                              size_t ws_size, hipStream_t stream) {
  fused_kernel<<<256, 512, 0, stream>>>((const char*)d_in[0], (const char*)d_in[1], (const char*)d_in[2],
                                        (const char*)d_in[3], (const char*)d_in[4], (char*)d_ws);
  reduce_kernel<<<256, 256, 0, stream>>>((char*)d_ws, (char*)d_out);
  spin_k<<<1, 256, 0, stream>>>((const unsigned long long*)((char*)d_ws + STAMP_OFS));
}
```

```cpp
#define STAMPS
#include <hip/hip_runtime.h>
#include <hip/hip_bf16.h>

typedef __bf16 bf16x4 __attribute__((ext_vector_type(4)));
typedef __bf16 bf16x8 __attribute__((ext_vector_type(8)));
typedef float f32x16 __attribute__((ext_vector_type(16)));
typedef float f32x2 __attribute__((ext_vector_type(2)));
typedef unsigned u32x4 __attribute__((ext_vector_type(4)));
#define LDSP __attribute__((address_space(3)))
#define GASP __attribute__((address_space(1)))
#define DI __device__ __forceinline__
#define MFMA(a, b, c) __builtin_amdgcn_mfma_f32_32x32x16_bf16((a), (b), (c), 0, 0, 0)
#define BAR() do { asm volatile("" ::: "memory"); __builtin_amdgcn_s_barrier(); asm volatile("" ::: "memory"); } while (0)
#define WAITV(n) asm volatile("s_waitcnt vmcnt(" #n ")" ::: "memory")
#define SB0() __builtin_amdgcn_sched_barrier(0)
#define WAITL0() do { asm volatile("" ::: "memory"); SB0(); __builtin_amdgcn_s_waitcnt(0xC07F); SB0(); asm volatile("" ::: "memory"); } while (0)
#define PAUX 16
#define LPRIO 0
#define KVAUX 2

constexpr int LDS_X = 0;
constexpr int LDS_K = 32768;
constexpr int LDS_W = 65536;
constexpr int LDS_V = 131072;
constexpr int LDS_TOTAL = 163840;

template <int AUX>
DI void dma16(const char* g, LDSP char* l) {
  __builtin_amdgcn_global_load_lds((const GASP unsigned*)g, (LDSP unsigned*)l, 16, 0, AUX);
}
DI bf16x8 tr_frag(LDSP char* p, int off_lo, int off_hi) {
  bf16x4 a = __builtin_amdgcn_ds_read_tr16_b64_v4bf16((LDSP bf16x4*)(p + off_lo));
  bf16x4 b = __builtin_amdgcn_ds_read_tr16_b64_v4bf16((LDSP bf16x4*)(p + off_hi));
  return __builtin_shufflevector(a, b, 0, 1, 2, 3, 4, 5, 6, 7);
}
DI bf16x8 pack8(const f32x16& x, int s) {
  bf16x8 p;
#pragma unroll
  for (int j = 0; j < 8; ++j) p[j] = (__bf16)x[8 * s + j];
  return p;
}

#define TS()
#define TSREC()
#define TSDUMP()
#define CBAR() BAR()
#define CWAITL0() WAITL0()
#define STAMP_OFS (128u << 20)
#define NSTAMPS 16
#define STAMP(k) do { if (wid == 0 && lane == 0) ((unsigned long long*)(part + STAMP_OFS))[bid * 64 + (k)] = __builtin_amdgcn_s_memrealtime(); } while (0)
struct FragSet { bf16x8 a[2][4]; bf16x8 b[2]; };

__global__ __launch_bounds__(512, 2) void fused_kernel(const char* __restrict__ x, const char* __restrict__ kc,
                                                       const char* __restrict__ vc, const char* __restrict__ wq,
                                                       const char* __restrict__ wo, char* __restrict__ part) {
  __shared__ __attribute__((aligned(16))) char smem_[LDS_TOTAL];
  LDSP char* smem = (LDSP char*)smem_;
  const int wid = __builtin_amdgcn_readfirstlane(threadIdx.x >> 6);
  const int lane = threadIdx.x & 63;
  const int bid = blockIdx.x;
  const int hp = (bid & 7) * 2 + ((bid >> 3) & 1);
  const int b = bid >> 4;
  const int u0 = ((b >> 1) + 4 * (hp & 1)) & 7;
  const int swp = b & 1;

  if (wid >= 4) {
    const int lw = wid - 4;
    const int hi = lane >> 5;
    const auto rq = __builtin_amdgcn_make_buffer_rsrc((void*)wq, 0, 4 << 20, 0x00020000);
    const auto ro = __builtin_amdgcn_make_buffer_rsrc((void*)wo, 0, 4 << 20, 0x00020000);
    const auto rx = __builtin_amdgcn_make_buffer_rsrc((void*)x, 0, 1 << 20, 0x00020000);
    const auto rk = __builtin_amdgcn_make_buffer_rsrc((void*)kc, 0, 8 << 20, 0x00020000);
    const auto rv = __builtin_amdgcn_make_buffer_rsrc((void*)vc, 0, 8 << 20, 0x00020000);
    const int wq_e = (8 * lw + hi) * 8192 + hp * 512 + (((lane & 31) ^ (hi << 2)) << 4);
    const int wq_o = (8 * lw + 2 + hi) * 8192 + hp * 512 + (((lane & 31) ^ ((2 + hi) << 2)) << 4);
    const int xg = b * 65536 + (8 * lw + (lane >> 3)) * 1024 + (((lane & 7) ^ (4 * (lw & 1) + (lane >> 4))) << 4);
    const int c16 = ((lane & 15) ^ ((lane >> 4) << 2)) << 4;
    const int wog = (hp * 256 + 16 * lw + (lane >> 4)) * 1024 + c16;
    const int kvb = (b * 32 + hp * 2) * 16384;
    const int vg = kvb + (16 * lw + (lane >> 4)) * 256 + c16;
    const int kg = kvb + (4 * lw + (lane >> 4)) * 256 + (((lane & 15) ^ (4 * lw + (lane >> 4))) << 4);
    const int wopf = (hp * 256 + b * 16 + lw * 4) * 1024 + lane * 64;
#define DMA(rs, vo, so, l, aux) __builtin_amdgcn_raw_ptr_buffer_load_lds(rs, (LDSP unsigned*)(l), 16, vo, so, 0, aux)
    __builtin_amdgcn_s_setprio(LPRIO);

    auto issue_wq = [&](int i) {
      const int ks = 2 * ((u0 + (i >> 1)) & 7) + ((i & 1) ^ swp);
      LDSP char* d = smem + LDS_W + (i & 3) * 16384 + lw * 4096;
      const int go = ks * 32 * 8192;
      DMA(rq, wq_e, go, d, 0);
      DMA(rq, wq_o, go, d + 1024, 0);
      DMA(rq, wq_e, go + 4 * 8192, d + 2048, 0);
      DMA(rq, wq_o, go + 4 * 8192, d + 3072, 0);
    };
    auto issue_x = [&](int u, int hf) {
      DMA(rx, xg, hf * 32768 + ((u0 + u) & 7) * 128, smem + LDS_X + (u & 3) * 8192 + hf * 4096 + lw * 1024, 0);
    };
    auto issue_wo = [&](int t) {
      LDSP char* d = smem + (t >= 8 ? t - 8 : t + 2) * 16384 + lw * 4096;
      const int go = (t & 3) * 64 * 1024 + (t >> 2) * 256;
      DMA(ro, wog, go, d, 0);
      DMA(ro, wog, go + 4096, d + 1024, 0);
      DMA(ro, wog, go + 8192, d + 2048, 0);
      DMA(ro, wog, go + 12288, d + 3072, 0);
    };
    auto issue_k = [&](int hd) {
#pragma unroll
      for (int i = 0; i < 4; ++i) DMA(rk, kg, hd * 16384 + i * 4096, smem + LDS_K + hd * 16384 + i * 4096 + lw * 1024, KVAUX);
    };
    auto issue_v = [&](int hd) {
#pragma unroll
      for (int i = 0; i < 4; ++i) DMA(rv, vg, hd * 16384 + i * 1024, smem + LDS_V + hd * 16384 + lw * 4096 + i * 1024, KVAUX);
    };
    issue_wq(0);
#pragma unroll
    for (int u = 0; u < 4; ++u) { issue_x(u, 0); issue_x(u, 1); }
    issue_k(0);
    int lb[3];
    lb[0] = lw * 4096 + lane * 16; lb[1] = lb[0] + 65536; lb[2] = lb[0] + 131072;
    asm volatile("" : "+v"(lb[0]), "+v"(lb[1]), "+v"(lb[2]));
    WAITV(8); BAR();
    issue_wq(1); issue_k(1); issue_wq(2);
    WAITV(8); BAR();
    issue_v(0); issue_wq(3);
    WAITV(8); BAR();
    issue_v(1); issue_wq(4);
    WAITV(8); BAR();
    issue_wq(5); issue_x(4, 0);
#pragma unroll 1
    for (int i = 6; i <= 12; ++i) {
      issue_wq(i); issue_x((i + 3) >> 1, (i + 1) & 1);
      WAITV(10); BAR();
    }
    issue_wq(13); WAITV(9); BAR();
    issue_wq(14); WAITV(8); BAR();
    issue_wq(15);
    __builtin_amdgcn_raw_ptr_buffer_load_lds(ro, (LDSP unsigned*)(smem + LDS_K + lw * 4096), 4, wopf, 0, 0, 0);
    issue_wo(0);
    WAITV(13); BAR();
    WAITV(9); BAR();
    WAITV(5); BAR();
    issue_wo(1);
    BAR();
#pragma unroll 1
    for (int t = 2; t < 6; ++t) issue_wo(t);
    u32x4 st[10][4];
#define GET(t) do { _Pragma("unroll") for (int i = 0; i < 4; ++i) \
        asm volatile("buffer_load_dwordx4 %0, %1, %2, %3 offen" : "=v"(st[(t) - 6][i]) : "v"(wog), "s"(ro), "s"(((t) & 3) * 64 * 1024 + ((t) >> 2) * 256 + i * 4096) : "memory"); } while (0)
#define PUT(t, n) do { asm volatile("s_waitcnt vmcnt(" #n ")" : "+v"(st[(t) - 6][0]), "+v"(st[(t) - 6][1]), "+v"(st[(t) - 6][2]), "+v"(st[(t) - 6][3]) :: "memory"); \
      _Pragma("unroll") for (int i = 0; i < 4; ++i) { constexpr int sl = ((t) >= 8 ? (t) - 8 : (t) + 2); \
        *(LDSP u32x4*)(smem + lb[sl >> 2] + (sl & 3) * 16384 + i * 1024) = st[(t) - 6][i]; } } while (0)
    GET(6); GET(7); GET(8); GET(9); GET(10);
    WAITV(20); BAR();
    GET(11); GET(12); GET(13);
    PUT(6, 28); PUT(7, 24);
    WAITL0(); BAR();
    GET(14); GET(15);
    PUT(8, 28); PUT(9, 24); PUT(10, 20); PUT(11, 16);
    WAITL0(); BAR();
    PUT(12, 12); PUT(13, 8); PUT(14, 4); PUT(15, 0);
    WAITL0(); BAR();
    return;
  }

  const int h = wid >> 1, qh = wid & 1;
  const int r = lane & 31, hh = lane >> 5;
  const int blk = (lane >> 4) & 1, q4 = (lane >> 2) & 3, p4 = lane & 3;
  const int pp = ((p4 & 1) << 1) | (p4 >> 1);
  const int pir = (r & 19) | ((r & 4) << 1) | ((r & 8) >> 1);
  STAMP(0);

  int woff[4];
#pragma unroll
  for (int dt = 0; dt < 4; ++dt) woff[dt] = LDS_W + (8 * hh + q4) * 512 + h * 256 + ((dt ^ q4) << 6) + blk * 32 + pp * 8;
  const int xrow = 32 * qh + r;
  int xoff[4];
#pragma unroll
  for (int ks = 0; ks < 4; ++ks) xoff[ks] = LDS_X + xrow * 128 + (((2 * (ks ^ (2 * swp)) + hh) ^ ((xrow >> 1) & 7)) << 4);

  int voff[4];
#pragma unroll
  for (int dt = 0; dt < 4; ++dt) voff[dt] = LDS_V + h * 16384 + (8 * hh + q4) * 256 + ((dt ^ q4) << 6) + blk * 32 + pp * 8;
  int ooff = LDS_X + (qh * 16 + h * 8) * 1024 + lane * 16;
  asm volatile("" : "+v"(voff[0]), "+v"(voff[1]), "+v"(voff[2]), "+v"(voff[3]), "+v"(ooff));
  f32x16 accq[4];
#pragma unroll
  for (int dt = 0; dt < 4; ++dt)
#pragma unroll
    for (int i = 0; i < 16; ++i) accq[dt][i] = 0.f;

  auto read_stage = [&](FragSet& f, int slot, int xu, int par) {
    LDSP char* wb = smem + slot * 16384;
    LDSP char* xb = smem + xu * 8192;
#pragma unroll
    for (int ks = 0; ks < 2; ++ks) {
      f.b[ks] = *(LDSP bf16x8*)(xb + xoff[2 * par + ks]);
#pragma unroll
      for (int dt = 0; dt < 4; ++dt) f.a[ks][dt] = tr_frag(wb + woff[dt], ks * 8192, ks * 8192 + 2048);
    }
  };
  auto mma_stage = [&](const FragSet& f) {
#pragma unroll
    for (int ks = 0; ks < 2; ++ks)
#pragma unroll
      for (int dt = 0; dt < 4; ++dt) accq[dt] = MFMA(f.a[ks][dt], f.b[ks], accq[dt]);
  };
#define INTERLEAVE(NM, ND) do { _Pragma("unroll") for (int z = 0; z < NM; ++z) { \
    __builtin_amdgcn_sched_group_barrier(0x8, 1, 0); __builtin_amdgcn_sched_group_barrier(0x100, ND, 0); } } while (0)

  bf16x8 kf[2][8];
  FragSet f0, f1;
  CBAR();
  STAMP(1);
  read_stage(f0, 0, 0, 0);
  CWAITL0();
#pragma unroll 1
  for (int u = 0; u < 7; ++u) {
    CBAR();
    read_stage(f1, (2 * u + 1) & 3, u & 3, 1);
    mma_stage(f0);
    INTERLEAVE(8, 3);
    CWAITL0();
    CBAR();
    read_stage(f0, (2 * u + 2) & 3, (u + 1) & 3, 0);
    mma_stage(f1);
    INTERLEAVE(8, 3);
    CWAITL0();
    if (u == 4) {
      LDSP char* kb = smem + LDS_K + h * 16384 + pir * 256;
#pragma unroll
      for (int kt = 0; kt < 2; ++kt)
#pragma unroll
        for (int s = 0; s < 8; ++s) kf[kt][s] = *(LDSP bf16x8*)(kb + kt * 8192 + (((2 * s + hh) ^ (pir & 15)) << 4));
      STAMP(12);
    }
  }
  CBAR();
  read_stage(f1, 3, 3, 1);
  mma_stage(f0);
  INTERLEAVE(8, 3);
  CWAITL0();
  CBAR();
  bf16x8 qf[8], vf[4][4], pf[4];
  f32x16 accs[2], acco[4];
  const f32x16 zero16 = {0.f, 0.f, 0.f, 0.f, 0.f, 0.f, 0.f, 0.f, 0.f, 0.f, 0.f, 0.f, 0.f, 0.f, 0.f, 0.f};
#define QM(dt, ks) accq[dt] = MFMA(f1.a[ks][dt], f1.b[ks], accq[dt])
#define QP(i) qf[i] = pack8(accq[(i) >> 1], (i) & 1)
#define SM(kt, s) accs[kt] = MFMA(kf[kt][s], qf[s], (s) == 0 ? zero16 : accs[kt])
#define VR(dt, s) vf[dt][s] = tr_frag(smem + voff[dt], (s) * 4096, (s) * 4096 + 1024)
  QM(0, 0); QM(0, 1); QM(1, 0); QM(1, 1); SB0();
  QP(0); SB0(); QM(2, 0); SB0();
  QP(1); SB0(); QM(2, 1); SB0();
  QP(2); SB0(); QM(3, 0); SB0();
  QP(3); SB0(); QM(3, 1); SB0();
  VR(0, 0); VR(1, 0); SB0(); SM(0, 0); SB0();
  QP(4); SB0(); SM(0, 1); SB0();
  QP(5); SB0(); SM(0, 2); SB0();
  QP(6); SB0(); SM(0, 3); SB0();
  QP(7); SB0(); SM(0, 4); SB0();
  VR(2, 0); VR(3, 0); SB0(); SM(0, 5); SB0();
  VR(0, 1); VR(1, 1); SB0(); SM(0, 6); SB0();
  VR(2, 1); VR(3, 1); SB0(); SM(0, 7); SB0();
  VR(0, 2); VR(0, 3); SB0(); SM(1, 0); SB0();
  VR(1, 2); VR(1, 3); SB0(); SM(1, 1); SB0();
  VR(2, 2); VR(2, 3); SB0(); SM(1, 2); SB0();
  VR(3, 2); VR(3, 3); SB0(); SM(1, 3); SB0();
  float mx = fmaxf(fmaxf(accs[0][0], accs[0][1]), accs[0][2]);
  mx = fmaxf(fmaxf(mx, accs[0][3]), accs[0][4]); mx = fmaxf(fmaxf(mx, accs[0][5]), accs[0][6]);
  SB0(); SM(1, 4); SB0();
  mx = fmaxf(fmaxf(mx, accs[0][7]), accs[0][8]); mx = fmaxf(fmaxf(mx, accs[0][9]), accs[0][10]); mx = fmaxf(fmaxf(mx, accs[0][11]), accs[0][12]);
  SB0(); SM(1, 5); SB0();
  mx = fmaxf(fmaxf(mx, accs[0][13]), accs[0][14]); mx = fmaxf(mx, accs[0][15]);
  SB0(); SM(1, 6); SM(1, 7); SB0();
  STAMP(2);
  {
    float m2 = fmaxf(fmaxf(accs[1][0], accs[1][1]), accs[1][2]);
#pragma unroll
    for (int i = 3; i < 9; i += 2) m2 = fmaxf(fmaxf(m2, accs[1][i]), accs[1][i + 1]);
#pragma unroll
    for (int i = 9; i < 15; i += 2) mx = fmaxf(fmaxf(mx, accs[1][i]), accs[1][i + 1]);
    mx = fmaxf(fmaxf(mx, m2), accs[1][15]);
    auto sw = __builtin_amdgcn_permlane32_swap(__float_as_uint(mx), __float_as_uint(mx), false, false);
    mx = fmaxf(__uint_as_float(sw[0]), __uint_as_float(sw[1]));
  }
  const float sc = 0.08838834764831845f * 1.4426950408889634f;
  const float mb = mx * sc;
  f32x2 sum2 = {0.f, 0.f}, sum3 = {0.f, 0.f};
#pragma unroll
  for (int kt = 0; kt < 2; ++kt)
#pragma unroll
    for (int i = 0; i < 16; i += 2) {
      f32x2 t = {accs[kt][i], accs[kt][i + 1]};
      t = t * sc - mb;
      t[0] = __builtin_amdgcn_exp2f(t[0]); t[1] = __builtin_amdgcn_exp2f(t[1]);
      accs[kt][i] = t[0]; accs[kt][i + 1] = t[1];
      if (i & 2) sum3 += t; else sum2 += t;
    }
  sum2 += sum3;
  float sum = sum2[0] + sum2[1];
  {
    auto sw = __builtin_amdgcn_permlane32_swap(__float_as_uint(sum), __float_as_uint(sum), false, false);
    sum = __uint_as_float(sw[0]) + __uint_as_float(sw[1]);
  }
  const float inv = __builtin_amdgcn_rcpf(sum);
  SB0();
#define PH(s, hf) do { _Pragma("unroll") for (int j = 4 * (hf); j < 4 * (hf) + 4; j += 2) { \
    f32x2 t = {accs[(s) >> 1][8 * ((s) & 1) + j], accs[(s) >> 1][8 * ((s) & 1) + j + 1]}; t *= inv; \
    pf[s][j] = (__bf16)t[0]; pf[s][j + 1] = (__bf16)t[1]; } } while (0)
#define OM(dt, s) acco[dt] = MFMA(vf[dt][s], pf[s], (s) == 0 ? zero16 : acco[dt])
#define OW(i) *(LDSP bf16x8*)(smem + ooff + (i) * 1024) = pack8(acco[(i) >> 1], (i) & 1)
  PH(0, 0); PH(0, 1); SB0();
  OM(0, 0); SB0(); PH(1, 0); SB0();
  OM(1, 0); SB0(); PH(1, 1); SB0();
  OM(2, 0); SB0(); PH(2, 0); SB0();
  OM(3, 0); SB0(); PH(2, 1); SB0();
  OM(0, 1); SB0(); PH(3, 0); SB0();
  OM(1, 1); SB0(); PH(3, 1); SB0();
  OM(2, 1); OM(3, 1); OM(0, 2); OM(0, 3); OM(1, 2); OM(1, 3); SB0();
  OW(0); SB0(); OM(2, 2); SB0();
  OW(1); SB0(); OM(2, 3); SB0();
  OW(2); SB0(); OM(3, 2); SB0();
  OW(3); SB0(); OM(3, 3); SB0();
  OW(4); OW(5); SB0(); OW(6); OW(7);
  CWAITL0();
  STAMP(3);
  CBAR();
  STAMP(4);

  int wob[3];
  wob[0] = (8 * hh + q4) * 256 + ((wid ^ q4) << 6) + blk * 32 + pp * 8; wob[1] = wob[0] + 65536; wob[2] = wob[0] + 131072;
  asm volatile("" : "+v"(wob[0]), "+v"(wob[1]), "+v"(wob[2]));
  bf16x8 wf[2][4];
  bf16x8 of[2][16];
  f32x16 acc[2][2];
  const auto prs = __builtin_amdgcn_make_buffer_rsrc(part, 0, 16 << 20, 0x00020000);
  const int pofs = ((b * 4 * 16 + 4 * wid + (r >> 4)) * 16 + hp) * 1024 + hh * 256 + (r & 15) * 16;
  auto read_wo = [&](int t) {
    const int sl = (t >= 8 ? t - 8 : t + 2);
    LDSP char* p = smem + wob[sl >> 2] + (sl & 3) * 16384;
#pragma unroll
    for (int ks = 0; ks < 4; ++ks) wf[t & 1][ks] = tr_frag(p, ks * 4096, ks * 4096 + 1024);
  };
  auto read_of = [&](int tq) {
#pragma unroll
    for (int ks = 4 * tq; ks < 4 * tq + 4; ++ks)
#pragma unroll
      for (int qt = 0; qt < 2; ++qt) of[qt][ks] = *(LDSP bf16x8*)(smem + LDS_X + (qt * 16 + ks) * 1024 + lane * 16);
  };
  auto store_chunk = [&](int c) {
#pragma unroll
    for (int qt = 0; qt < 2; ++qt)
#pragma unroll
      for (int a = 0; a < 2; ++a)
        __builtin_amdgcn_raw_buffer_store_b128(__builtin_bit_cast(u32x4, pack8(acc[c & 1][qt], a)), prs,
                                               pofs + (c * 16 + 2 * qt) * 16384 + a * 512, 0, PAUX);
  };
  read_wo(0); read_of(0);
  SB0();
#pragma unroll
  for (int t = 0; t < 16; ++t) {
    const int c = t >> 2, tq = t & 3;
    if (t == 2 || t == 6 || t == 11) { CWAITL0(); CBAR(); }
    if (t < 15) read_wo(t + 1);
    if (t == 0) { read_of(1); read_of(2); }
    if (t == 1) read_of(3);
    if (tq == 0) { acc[c & 1][0] = zero16; acc[c & 1][1] = zero16; }
#pragma unroll
    for (int ks = 0; ks < 4; ++ks)
#pragma unroll
      for (int qt = 0; qt < 2; ++qt) acc[c & 1][qt] = MFMA(wf[t & 1][ks], of[qt][4 * tq + ks], acc[c & 1][qt]);
    if (tq == 0 && c > 0) store_chunk(c - 1);
    if (t == 0) { INTERLEAVE(8, 3); }
    else if (t == 1) { INTERLEAVE(8, 2); }
    else if (tq == 0) {
      INTERLEAVE(4, 2);
#pragma unroll
      for (int z = 0; z < 4; ++z) { __builtin_amdgcn_sched_group_barrier(0x8, 1, 0); __builtin_amdgcn_sched_group_barrier(0x2, 5, 0); __builtin_amdgcn_sched_group_barrier(0x40, 1, 0); }
    } else { INTERLEAVE(4, 2); }
    SB0();
    if (tq == 3) STAMP(5 + c);
  }
  store_chunk(3);
  WAITV(0);
  STAMP(9);
  TS(); CWAITL0(); WAITV(0); TS(); CWAITL0(); TSDUMP();
}

__global__ __launch_bounds__(256) void reduce_kernel(char* __restrict__ part, char* __restrict__ out) {
  const int bid = blockIdx.x, lane = threadIdx.x & 63, c = threadIdx.x >> 6;
  const int b = bid >> 4, j = bid & 15;
  const int wid = c;
  STAMP(10);
  const char* p = part + (size_t)((b * 4 + c) * 16 + j) * 16384 + lane * 16;
  u32x4 v[16];
#pragma unroll
  for (int i = 0; i < 16; ++i) v[i] = *(const u32x4*)(p + i * 1024);
  float s[8];
#pragma unroll
  for (int e = 0; e < 8; ++e) s[e] = 0.f;
#pragma unroll
  for (int i = 0; i < 16; ++i)
#pragma unroll
    for (int e = 0; e < 4; ++e) {
      s[2 * e] += __uint_as_float(v[i][e] << 16);
      s[2 * e + 1] += __uint_as_float(v[i][e] & 0xffff0000u);
    }
  bf16x8 o;
#pragma unroll
  for (int e = 0; e < 8; ++e) o[e] = (__bf16)s[e];
  const int w = j >> 2, qt = (j >> 1) & 1, hf = j & 1;
  const int row = b * 64 + 32 * qt + 16 * hf + (lane & 15);
  const int col = 128 * c + 32 * w + 16 * (lane >> 5) + 8 * ((lane >> 4) & 1);
  *(bf16x8*)(out + (size_t)row * 1024 + col * 2) = o;
  WAITV(0);
  STAMP(11);
}

__global__ void k0(unsigned long long* st) { if (threadIdx.x == 0) st[63] = __builtin_amdgcn_s_memrealtime(); }
__global__ void spin_k(const unsigned long long* st, int mode) {
  unsigned long long tin = __builtin_amdgcn_s_memrealtime();
  __shared__ unsigned long long sa[256], sb[256];
  int t = threadIdx.x;
  sa[t] = st[t * 64]; sb[t] = st[t * 64 + 11]; __syncthreads();
  if (t == 0) {
    unsigned long long mn = ~0ull, mx = 0;
    for (int i = 0; i < 256; ++i) { mn = sa[i] < mn ? sa[i] : mn; mx = sb[i] > mx ? sb[i] : mx; }
    unsigned long long span = mode == 0 ? mn - st[63] : (mode == 1 ? tin - mx : 0); if (span > 10000) span = 10000;
    unsigned long long t0 = __builtin_amdgcn_s_memrealtime();
    while (__builtin_amdgcn_s_memrealtime() - t0 < span * 50) __builtin_amdgcn_s_sleep(1);
  }
}
extern "C" void kernel_launch(void* const* d_in, const int* in_sizes, int n_in, void* d_out, int out_size, void* d_ws,
                              size_t ws_size, hipStream_t stream) {
  fused_kernel<<<256, 512, 0, stream>>>((const char*)d_in[0], (const char*)d_in[1], (const char*)d_in[2],
                                        (const char*)d_in[3], (const char*)d_in[4], (char*)d_ws);
  reduce_kernel<<<256, 256, 0, stream>>>((char*)d_ws, (char*)d_out);
}
```

```cpp
#define STAMPS
#include <hip/hip_runtime.h>
#include <hip/hip_bf16.h>

typedef __bf16 bf16x4 __attribute__((ext_vector_type(4)));
typedef __bf16 bf16x8 __attribute__((ext_vector_type(8)));
typedef float f32x16 __attribute__((ext_vector_type(16)));
typedef float f32x2 __attribute__((ext_vector_type(2)));
typedef unsigned u32x4 __attribute__((ext_vector_type(4)));
#define LDSP __attribute__((address_space(3)))
#define GASP __attribute__((address_space(1)))
#define DI __device__ __forceinline__
#define MFMA(a, b, c) __builtin_amdgcn_mfma_f32_32x32x16_bf16((a), (b), (c), 0, 0, 0)
#define BAR() do { asm volatile("" ::: "memory"); __builtin_amdgcn_s_barrier(); asm volatile("" ::: "memory"); } while (0)
#define WAITV(n) asm volatile("s_waitcnt vmcnt(" #n ")" ::: "memory")
#define SB0() __builtin_amdgcn_sched_barrier(0)
#define WAITL0() do { asm volatile("" ::: "memory"); SB0(); __builtin_amdgcn_s_waitcnt(0xC07F); SB0(); asm volatile("" ::: "memory"); } while (0)
#define PAUX 16
#define LPRIO 0
#define KVAUX 2

constexpr int LDS_X = 0;
constexpr int LDS_K = 32768;
constexpr int LDS_W = 65536;
constexpr int LDS_V = 131072;
constexpr int LDS_TOTAL = 163840;

template <int AUX>
DI void dma16(const char* g, LDSP char* l) {
  __builtin_amdgcn_global_load_lds((const GASP unsigned*)g, (LDSP unsigned*)l, 16, 0, AUX);
}
DI bf16x8 tr_frag(LDSP char* p, int off_lo, int off_hi) {
  bf16x4 a = __builtin_amdgcn_ds_read_tr16_b64_v4bf16((LDSP bf16x4*)(p + off_lo));
  bf16x4 b = __builtin_amdgcn_ds_read_tr16_b64_v4bf16((LDSP bf16x4*)(p + off_hi));
  return __builtin_shufflevector(a, b, 0, 1, 2, 3, 4, 5, 6, 7);
}
DI bf16x8 pack8(const f32x16& x, int s) {
  bf16x8 p;
#pragma unroll
  for (int j = 0; j < 8; ++j) p[j] = (__bf16)x[8 * s + j];
  return p;
}

#define TS()
#define TSREC()
#define TSDUMP()
#define CBAR() BAR()
#define CWAITL0() WAITL0()
#define STAMP_OFS (128u << 20)
#define NSTAMPS 16
#define STAMP(k) do { if (wid == 0 && lane == 0) ((unsigned long long*)(part + STAMP_OFS))[bid * 64 + (k)] = __builtin_amdgcn_s_memrealtime(); } while (0)
struct FragSet { bf16x8 a[2][4]; bf16x8 b[2]; };

__global__ __launch_bounds__(512, 2) void fused_kernel(const char* __restrict__ x, const char* __restrict__ kc,
                                                       const char* __restrict__ vc, const char* __restrict__ wq,
                                                       const char* __restrict__ wo, char* __restrict__ part) {
  __shared__ __attribute__((aligned(16))) char smem_[LDS_TOTAL];
  LDSP char* smem = (LDSP char*)smem_;
  const int wid = __builtin_amdgcn_readfirstlane(threadIdx.x >> 6);
  const int lane = threadIdx.x & 63;
  const int bid = blockIdx.x;
  const int hp = (bid & 7) * 2 + ((bid >> 3) & 1);
  const int b = bid >> 4;
  const int u0 = ((b >> 1) + 4 * (hp & 1)) & 7;
  const int swp = b & 1;

  if (wid >= 4) {
    const int lw = wid - 4;
    const int hi = lane >> 5;
    const auto rq = __builtin_amdgcn_make_buffer_rsrc((void*)wq, 0, 4 << 20, 0x00020000);
    const auto ro = __builtin_amdgcn_make_buffer_rsrc((void*)wo, 0, 4 << 20, 0x00020000);
    const auto rx = __builtin_amdgcn_make_buffer_rsrc((void*)x, 0, 1 << 20, 0x00020000);
    const auto rk = __builtin_amdgcn_make_buffer_rsrc((void*)kc, 0, 8 << 20, 0x00020000);
    const auto rv = __builtin_amdgcn_make_buffer_rsrc((void*)vc, 0, 8 << 20, 0x00020000);
    const int wq_e = (8 * lw + hi) * 8192 + hp * 512 + (((lane & 31) ^ (hi << 2)) << 4);
    const int wq_o = (8 * lw + 2 + hi) * 8192 + hp * 512 + (((lane & 31) ^ ((2 + hi) << 2)) << 4);
    const int xg = b * 65536 + (8 * lw + (lane >> 3)) * 1024 + (((lane & 7) ^ (4 * (lw & 1) + (lane >> 4))) << 4);
    const int c16 = ((lane & 15) ^ ((lane >> 4) << 2)) << 4;
    const int wog = (hp * 256 + 16 * lw + (lane >> 4)) * 1024 + c16;
    const int kvb = (b * 32 + hp * 2) * 16384;
    const int vg = kvb + (16 * lw + (lane >> 4)) * 256 + c16;
    const int kg = kvb + (4 * lw + (lane >> 4)) * 256 + (((lane & 15) ^ (4 * lw + (lane >> 4))) << 4);
    const int wopf = (hp * 256 + b * 16 + lw * 4) * 1024 + lane * 64;
#define DMA(rs, vo, so, l, aux) __builtin_amdgcn_raw_ptr_buffer_load_lds(rs, (LDSP unsigned*)(l), 16, vo, so, 0, aux)
    __builtin_amdgcn_s_setprio(LPRIO);

    auto issue_wq = [&](int i) {
      const int ks = 2 * ((u0 + (i >> 1)) & 7) + ((i & 1) ^ swp);
      LDSP char* d = smem + LDS_W + (i & 3) * 16384 + lw * 4096;
      const int go = ks * 32 * 8192;
      DMA(rq, wq_e, go, d, 0);
      DMA(rq, wq_o, go, d + 1024, 0);
      DMA(rq, wq_e, go + 4 * 8192, d + 2048, 0);
      DMA(rq, wq_o, go + 4 * 8192, d + 3072, 0);
    };
    auto issue_x = [&](int u, int hf) {
      DMA(rx, xg, hf * 32768 + ((u0 + u) & 7) * 128, smem + LDS_X + (u & 3) * 8192 + hf * 4096 + lw * 1024, 0);
    };
    auto issue_wo = [&](int t) {
      LDSP char* d = smem + (t >= 8 ? t - 8 : t + 2) * 16384 + lw * 4096;
      const int go = (t & 3) * 64 * 1024 + (t >> 2) * 256;
      DMA(ro, wog, go, d, 0);
      DMA(ro, wog, go + 4096, d + 1024, 0);
      DMA(ro, wog, go + 8192, d + 2048, 0);
      DMA(ro, wog, go + 12288, d + 3072, 0);
    };
    auto issue_k = [&](int hd) {
#pragma unroll
      for (int i = 0; i < 4; ++i) DMA(rk, kg, hd * 16384 + i * 4096, smem + LDS_K + hd * 16384 + i * 4096 + lw * 1024, KVAUX);
    };
    auto issue_v = [&](int hd) {
#pragma unroll
      for (int i = 0; i < 4; ++i) DMA(rv, vg, hd * 16384 + i * 1024, smem + LDS_V + hd * 16384 + lw * 4096 + i * 1024, KVAUX);
    };
    issue_wq(0);
#pragma unroll
    for (int u = 0; u < 4; ++u) { issue_x(u, 0); issue_x(u, 1); }
    issue_k(0);
    int lb[3];
    lb[0] = lw * 4096 + lane * 16; lb[1] = lb[0] + 65536; lb[2] = lb[0] + 131072;
    asm volatile("" : "+v"(lb[0]), "+v"(lb[1]), "+v"(lb[2]));
    WAITV(8); BAR();
    issue_wq(1); issue_k(1); issue_wq(2);
    WAITV(8); BAR();
    issue_v(0); issue_wq(3);
    WAITV(8); BAR();
    issue_v(1); issue_wq(4);
    WAITV(8); BAR();
    issue_wq(5); issue_x(4, 0);
#pragma unroll 1
    for (int i = 6; i <= 12; ++i) {
      issue_wq(i); issue_x((i + 3) >> 1, (i + 1) & 1);
      WAITV(10); BAR();
    }
    issue_wq(13); WAITV(9); BAR();
    issue_wq(14); WAITV(8); BAR();
    issue_wq(15);
    __builtin_amdgcn_raw_ptr_buffer_load_lds(ro, (LDSP unsigned*)(smem + LDS_K + lw * 4096), 4, wopf, 0, 0, 0);
    issue_wo(0);
    WAITV(13); BAR();
    WAITV(9); BAR();
    WAITV(5); BAR();
    issue_wo(1);
    BAR();
#pragma unroll 1
    for (int t = 2; t < 6; ++t) issue_wo(t);
    u32x4 st[10][4];
#define GET(t) do { _Pragma("unroll") for (int i = 0; i < 4; ++i) \
        asm volatile("buffer_load_dwordx4 %0, %1, %2, %3 offen" : "=v"(st[(t) - 6][i]) : "v"(wog), "s"(ro), "s"(((t) & 3) * 64 * 1024 + ((t) >> 2) * 256 + i * 4096) : "memory"); } while (0)
#define PUT(t, n) do { asm volatile("s_waitcnt vmcnt(" #n ")" : "+v"(st[(t) - 6][0]), "+v"(st[(t) - 6][1]), "+v"(st[(t) - 6][2]), "+v"(st[(t) - 6][3]) :: "memory"); \
      _Pragma("unroll") for (int i = 0; i < 4; ++i) { constexpr int sl = ((t) >= 8 ? (t) - 8 : (t) + 2); \
        *(LDSP u32x4*)(smem + lb[sl >> 2] + (sl & 3) * 16384 + i * 1024) = st[(t) - 6][i]; } } while (0)
    GET(6); GET(7); GET(8); GET(9); GET(10);
    WAITV(20); BAR();
    GET(11); GET(12); GET(13);
    PUT(6, 28); PUT(7, 24);
    WAITL0(); BAR();
    GET(14); GET(15);
    PUT(8, 28); PUT(9, 24); PUT(10, 20); PUT(11, 16);
    WAITL0(); BAR();
    PUT(12, 12); PUT(13, 8); PUT(14, 4); PUT(15, 0);
    WAITL0(); BAR();
    return;
  }

  const int h = wid >> 1, qh = wid & 1;
  const int r = lane & 31, hh = lane >> 5;
  const int blk = (lane >> 4) & 1, q4 = (lane >> 2) & 3, p4 = lane & 3;
  const int pp = ((p4 & 1) << 1) | (p4 >> 1);
  const int pir = (r & 19) | ((r & 4) << 1) | ((r & 8) >> 1);
  STAMP(0);

  int woff[4];
#pragma unroll
  for (int dt = 0; dt < 4; ++dt) woff[dt] = LDS_W + (8 * hh + q4) * 512 + h * 256 + ((dt ^ q4) << 6) + blk * 32 + pp * 8;
  const int xrow = 32 * qh + r;
  int xoff[4];
#pragma unroll
  for (int ks = 0; ks < 4; ++ks) xoff[ks] = LDS_X + xrow * 128 + (((2 * (ks ^ (2 * swp)) + hh) ^ ((xrow >> 1) & 7)) << 4);

  int voff[4];
#pragma unroll
  for (int dt = 0; dt < 4; ++dt) voff[dt] = LDS_V + h * 16384 + (8 * hh + q4) * 256 + ((dt ^ q4) << 6) + blk * 32 + pp * 8;
  int ooff = LDS_X + (qh * 16 + h * 8) * 1024 + lane * 16;
  asm volatile("" : "+v"(voff[0]), "+v"(voff[1]), "+v"(voff[2]), "+v"(voff[3]), "+v"(ooff));
  f32x16 accq[4];
#pragma unroll
  for (int dt = 0; dt < 4; ++dt)
#pragma unroll
    for (int i = 0; i < 16; ++i) accq[dt][i] = 0.f;

  auto read_stage = [&](FragSet& f, int slot, int xu, int par) {
    LDSP char* wb = smem + slot * 16384;
    LDSP char* xb = smem + xu * 8192;
#pragma unroll
    for (int ks = 0; ks < 2; ++ks) {
      f.b[ks] = *(LDSP bf16x8*)(xb + xoff[2 * par + ks]);
#pragma unroll
      for (int dt = 0; dt < 4; ++dt) f.a[ks][dt] = tr_frag(wb + woff[dt], ks * 8192, ks * 8192 + 2048);
    }
  };
  auto mma_stage = [&](const FragSet& f) {
#pragma unroll
    for (int ks = 0; ks < 2; ++ks)
#pragma unroll
      for (int dt = 0; dt < 4; ++dt) accq[dt] = MFMA(f.a[ks][dt], f.b[ks], accq[dt]);
  };
#define INTERLEAVE(NM, ND) do { _Pragma("unroll") for (int z = 0; z < NM; ++z) { \
    __builtin_amdgcn_sched_group_barrier(0x8, 1, 0); __builtin_amdgcn_sched_group_barrier(0x100, ND, 0); } } while (0)

  bf16x8 kf[2][8];
  FragSet f0, f1;
  CBAR();
  STAMP(1);
  read_stage(f0, 0, 0, 0);
  CWAITL0();
#pragma unroll 1
  for (int u = 0; u < 7; ++u) {
    CBAR();
    read_stage(f1, (2 * u + 1) & 3, u & 3, 1);
    mma_stage(f0);
    INTERLEAVE(8, 3);
    CWAITL0();
    CBAR();
    read_stage(f0, (2 * u + 2) & 3, (u + 1) & 3, 0);
    mma_stage(f1);
    INTERLEAVE(8, 3);
    CWAITL0();
    if (u == 4) {
      LDSP char* kb = smem + LDS_K + h * 16384 + pir * 256;
#pragma unroll
      for (int kt = 0; kt < 2; ++kt)
#pragma unroll
        for (int s = 0; s < 8; ++s) kf[kt][s] = *(LDSP bf16x8*)(kb + kt * 8192 + (((2 * s + hh) ^ (pir & 15)) << 4));
      STAMP(12);
    }
  }
  CBAR();
  read_stage(f1, 3, 3, 1);
  mma_stage(f0);
  INTERLEAVE(8, 3);
  CWAITL0();
  CBAR();
  bf16x8 qf[8], vf[4][4], pf[4];
  f32x16 accs[2], acco[4];
  const f32x16 zero16 = {0.f, 0.f, 0.f, 0.f, 0.f, 0.f, 0.f, 0.f, 0.f, 0.f, 0.f, 0.f, 0.f, 0.f, 0.f, 0.f};
#define QM(dt, ks) accq[dt] = MFMA(f1.a[ks][dt], f1.b[ks], accq[dt])
#define QP(i) qf[i] = pack8(accq[(i) >> 1], (i) & 1)
#define SM(kt, s) accs[kt] = MFMA(kf[kt][s], qf[s], (s) == 0 ? zero16 : accs[kt])
#define VR(dt, s) vf[dt][s] = tr_frag(smem + voff[dt], (s) * 4096, (s) * 4096 + 1024)
  QM(0, 0); QM(0, 1); QM(1, 0); QM(1, 1); SB0();
  QP(0); SB0(); QM(2, 0); SB0();
  QP(1); SB0(); QM(2, 1); SB0();
  QP(2); SB0(); QM(3, 0); SB0();
  QP(3); SB0(); QM(3, 1); SB0();
  VR(0, 0); VR(1, 0); SB0(); SM(0, 0); SB0();
  QP(4); SB0(); SM(0, 1); SB0();
  QP(5); SB0(); SM(0, 2); SB0();
  QP(6); SB0(); SM(0, 3); SB0();
  QP(7); SB0(); SM(0, 4); SB0();
  VR(2, 0); VR(3, 0); SB0(); SM(0, 5); SB0();
  VR(0, 1); VR(1, 1); SB0(); SM(0, 6); SB0();
  VR(2, 1); VR(3, 1); SB0(); SM(0, 7); SB0();
  VR(0, 2); VR(0, 3); SB0(); SM(1, 0); SB0();
  VR(1, 2); VR(1, 3); SB0(); SM(1, 1); SB0();
  VR(2, 2); VR(2, 3); SB0(); SM(1, 2); SB0();
  VR(3, 2); VR(3, 3); SB0(); SM(1, 3); SB0();
  float mx = fmaxf(fmaxf(accs[0][0], accs[0][1]), accs[0][2]);
  mx = fmaxf(fmaxf(mx, accs[0][3]), accs[0][4]); mx = fmaxf(fmaxf(mx, accs[0][5]), accs[0][6]);
  SB0(); SM(1, 4); SB0();
  mx = fmaxf(fmaxf(mx, accs[0][7]), accs[0][8]); mx = fmaxf(fmaxf(mx, accs[0][9]), accs[0][10]); mx = fmaxf(fmaxf(mx, accs[0][11]), accs[0][12]);
  SB0(); SM(1, 5); SB0();
  mx = fmaxf(fmaxf(mx, accs[0][13]), accs[0][14]); mx = fmaxf(mx, accs[0][15]);
  SB0(); SM(1, 6); SM(1, 7); SB0();
  STAMP(2);
  {
    float m2 = fmaxf(fmaxf(accs[1][0], accs[1][1]), accs[1][2]);
#pragma unroll
    for (int i = 3; i < 9; i += 2) m2 = fmaxf(fmaxf(m2, accs[1][i]), accs[1][i + 1]);
#pragma unroll
    for (int i = 9; i < 15; i += 2) mx = fmaxf(fmaxf(mx, accs[1][i]), accs[1][i + 1]);
    mx = fmaxf(fmaxf(mx, m2), accs[1][15]);
    auto sw = __builtin_amdgcn_permlane32_swap(__float_as_uint(mx), __float_as_uint(mx), false, false);
    mx = fmaxf(__uint_as_float(sw[0]), __uint_as_float(sw[1]));
  }
  const float sc = 0.08838834764831845f * 1.4426950408889634f;
  const float mb = mx * sc;
  f32x2 sum2 = {0.f, 0.f}, sum3 = {0.f, 0.f};
#pragma unroll
  for (int kt = 0; kt < 2; ++kt)
#pragma unroll
    for (int i = 0; i < 16; i += 2) {
      f32x2 t = {accs[kt][i], accs[kt][i + 1]};
      t = t * sc - mb;
      t[0] = __builtin_amdgcn_exp2f(t[0]); t[1] = __builtin_amdgcn_exp2f(t[1]);
      accs[kt][i] = t[0]; accs[kt][i + 1] = t[1];
      if (i & 2) sum3 += t; else sum2 += t;
    }
  sum2 += sum3;
  float sum = sum2[0] + sum2[1];
  {
    auto sw = __builtin_amdgcn_permlane32_swap(__float_as_uint(sum), __float_as_uint(sum), false, false);
    sum = __uint_as_float(sw[0]) + __uint_as_float(sw[1]);
  }
  const float inv = __builtin_amdgcn_rcpf(sum);
  SB0();
#define PH(s, hf) do { _Pragma("unroll") for (int j = 4 * (hf); j < 4 * (hf) + 4; j += 2) { \
    f32x2 t = {accs[(s) >> 1][8 * ((s) & 1) + j], accs[(s) >> 1][8 * ((s) & 1) + j + 1]}; t *= inv; \
    pf[s][j] = (__bf16)t[0]; pf[s][j + 1] = (__bf16)t[1]; } } while (0)
#define OM(dt, s) acco[dt] = MFMA(vf[dt][s], pf[s], (s) == 0 ? zero16 : acco[dt])
#define OW(i) *(LDSP bf16x8*)(smem + ooff + (i) * 1024) = pack8(acco[(i) >> 1], (i) & 1)
  PH(0, 0); PH(0, 1); SB0();
  OM(0, 0); SB0(); PH(1, 0); SB0();
  OM(1, 0); SB0(); PH(1, 1); SB0();
  OM(2, 0); SB0(); PH(2, 0); SB0();
  OM(3, 0); SB0(); PH(2, 1); SB0();
  OM(0, 1); SB0(); PH(3, 0); SB0();
  OM(1, 1); SB0(); PH(3, 1); SB0();
  OM(2, 1); OM(3, 1); OM(0, 2); OM(0, 3); OM(1, 2); OM(1, 3); SB0();
  OW(0); SB0(); OM(2, 2); SB0();
  OW(1); SB0(); OM(2, 3); SB0();
  OW(2); SB0(); OM(3, 2); SB0();
  OW(3); SB0(); OM(3, 3); SB0();
  OW(4); OW(5); SB0(); OW(6); OW(7);
  CWAITL0();
  STAMP(3);
  CBAR();
  STAMP(4);

  int wob[3];
  wob[0] = (8 * hh + q4) * 256 + ((wid ^ q4) << 6) + blk * 32 + pp * 8; wob[1] = wob[0] + 65536; wob[2] = wob[0] + 131072;
  asm volatile("" : "+v"(wob[0]), "+v"(wob[1]), "+v"(wob[2]));
  bf16x8 wf[2][4];
  bf16x8 of[2][16];
  f32x16 acc[2][2];
  const auto prs = __builtin_amdgcn_make_buffer_rsrc(part, 0, 16 << 20, 0x00020000);
  const int pofs = ((b * 4 * 16 + 4 * wid + (r >> 4)) * 16 + hp) * 1024 + hh * 256 + (r & 15) * 16;
  auto read_wo = [&](int t) {
    const int sl = (t >= 8 ? t - 8 : t + 2);
    LDSP char* p = smem + wob[sl >> 2] + (sl & 3) * 16384;
#pragma unroll
    for (int ks = 0; ks < 4; ++ks) wf[t & 1][ks] = tr_frag(p, ks * 4096, ks * 4096 + 1024);
  };
  auto read_of = [&](int tq) {
#pragma unroll
    for (int ks = 4 * tq; ks < 4 * tq + 4; ++ks)
#pragma unroll
      for (int qt = 0; qt < 2; ++qt) of[qt][ks] = *(LDSP bf16x8*)(smem + LDS_X + (qt * 16 + ks) * 1024 + lane * 16);
  };
  auto store_chunk = [&](int c) {
#pragma unroll
    for (int qt = 0; qt < 2; ++qt)
#pragma unroll
      for (int a = 0; a < 2; ++a)
        __builtin_amdgcn_raw_buffer_store_b128(__builtin_bit_cast(u32x4, pack8(acc[c & 1][qt], a)), prs,
                                               pofs + (c * 16 + 2 * qt) * 16384 + a * 512, 0, PAUX);
  };
  read_wo(0); read_of(0);
  SB0();
#pragma unroll
  for (int t = 0; t < 16; ++t) {
    const int c = t >> 2, tq = t & 3;
    if (t == 2 || t == 6 || t == 11) { CWAITL0(); CBAR(); }
    if (t < 15) read_wo(t + 1);
    if (t == 0) { read_of(1); read_of(2); }
    if (t == 1) read_of(3);
    if (tq == 0) { acc[c & 1][0] = zero16; acc[c & 1][1] = zero16; }
#pragma unroll
    for (int ks = 0; ks < 4; ++ks)
#pragma unroll
      for (int qt = 0; qt < 2; ++qt) acc[c & 1][qt] = MFMA(wf[t & 1][ks], of[qt][4 * tq + ks], acc[c & 1][qt]);
    if (tq == 0 && c > 0) store_chunk(c - 1);
    if (t == 0) { INTERLEAVE(8, 3); }
    else if (t == 1) { INTERLEAVE(8, 2); }
    else if (tq == 0) {
      INTERLEAVE(4, 2);
#pragma unroll
      for (int z = 0; z < 4; ++z) { __builtin_amdgcn_sched_group_barrier(0x8, 1, 0); __builtin_amdgcn_sched_group_barrier(0x2, 5, 0); __builtin_amdgcn_sched_group_barrier(0x40, 1, 0); }
    } else { INTERLEAVE(4, 2); }
    SB0();
    if (tq == 3) STAMP(5 + c);
  }
  store_chunk(3);
  WAITV(0);
  STAMP(9);
  TS(); CWAITL0(); WAITV(0); TS(); CWAITL0(); TSDUMP();
}

__global__ __launch_bounds__(256) void reduce_kernel(char* __restrict__ part, char* __restrict__ out) {
  const int bid = blockIdx.x, lane = threadIdx.x & 63, c = threadIdx.x >> 6;
  const int b = bid >> 4, j = bid & 15;
  const int wid = c;
  STAMP(10);
  const char* p = part + (size_t)((b * 4 + c) * 16 + j) * 16384 + lane * 16;
  u32x4 v[16];
#pragma unroll
  for (int i = 0; i < 16; ++i) v[i] = *(const u32x4*)(p + i * 1024);
  float s[8];
#pragma unroll
  for (int e = 0; e < 8; ++e) s[e] = 0.f;
#pragma unroll
  for (int i = 0; i < 16; ++i)
#pragma unroll
    for (int e = 0; e < 4; ++e) {
      s[2 * e] += __uint_as_float(v[i][e] << 16);
      s[2 * e + 1] += __uint_as_float(v[i][e] & 0xffff0000u);
    }
  bf16x8 o;
#pragma unroll
  for (int e = 0; e < 8; ++e) o[e] = (__bf16)s[e];
  const int w = j >> 2, qt = (j >> 1) & 1, hf = j & 1;
  const int row = b * 64 + 32 * qt + 16 * hf + (lane & 15);
  const int col = 128 * c + 32 * w + 16 * (lane >> 5) + 8 * ((lane >> 4) & 1);
  *(bf16x8*)(out + (size_t)row * 1024 + col * 2) = o;
  WAITV(0);
  STAMP(11);
}

__global__ void k0(unsigned long long* st, int idx) { unsigned long long t = __builtin_amdgcn_s_memrealtime(); if (threadIdx.x == 0 && blockIdx.x == 0) st[idx] = t; }
__global__ void kd(unsigned long long* st, int idx) { unsigned long long t = __builtin_amdgcn_s_memrealtime(); while (__builtin_amdgcn_s_memrealtime() - t < 1000) __builtin_amdgcn_s_sleep(1); if (threadIdx.x == 0 && blockIdx.x == 0) st[idx] = __builtin_amdgcn_s_memrealtime(); }
__global__ void spin_k(const unsigned long long* st, int mode) {
  __shared__ unsigned long long sa[256];
  int t = threadIdx.x;
  sa[t] = st[t * 64]; __syncthreads();
  if (t == 0) {
    unsigned long long mn = ~0ull;
    for (int i = 0; i < 256; ++i) { mn = sa[i] < mn ? sa[i] : mn; }
    unsigned long long span = mode == 0 ? st[63] - st[62] : (mode == 1 ? mn - st[63] : 0); if (span > 10000) span = 10000;
    unsigned long long t0 = __builtin_amdgcn_s_memrealtime();
    while (__builtin_amdgcn_s_memrealtime() - t0 < span * 50) __builtin_amdgcn_s_sleep(1);
  }
}
extern "C" void kernel_launch(void* const* d_in, const int* in_sizes, int n_in, void* d_out, int out_size, void* d_ws,
                              size_t ws_size, hipStream_t stream) {
  k0<<<1, 64, 0, stream>>>((unsigned long long*)((char*)d_ws + STAMP_OFS), 62);
  k0<<<1, 64, 0, stream>>>((unsigned long long*)((char*)d_ws + STAMP_OFS), 63);
  fused_kernel<<<256, 512, 0, stream>>>((const char*)d_in[0], (const char*)d_in[1], (const char*)d_in[2],
                                        (const char*)d_in[3], (const char*)d_in[4], (char*)d_ws);
  reduce_kernel<<<256, 256, 0, stream>>>((char*)d_ws, (char*)d_out);
  spin_k<<<1, 256, 0, stream>>>((const unsigned long long*)((char*)d_ws + STAMP_OFS), 2);
}
```

```cpp
#include <hip/hip_runtime.h>
#include <hip/hip_bf16.h>

typedef __bf16 bf16x4 __attribute__((ext_vector_type(4)));
typedef __bf16 bf16x8 __attribute__((ext_vector_type(8)));
typedef float f32x16 __attribute__((ext_vector_type(16)));
typedef float f32x2 __attribute__((ext_vector_type(2)));
typedef unsigned u32x4 __attribute__((ext_vector_type(4)));
#define LDSP __attribute__((address_space(3)))
#define GASP __attribute__((address_space(1)))
#define DI __device__ __forceinline__
#define MFMA(a, b, c) __builtin_amdgcn_mfma_f32_32x32x16_bf16((a), (b), (c), 0, 0, 0)
#define BAR() do { asm volatile("" ::: "memory"); __builtin_amdgcn_s_barrier(); asm volatile("" ::: "memory"); } while (0)
#define WAITV(n) asm volatile("s_waitcnt vmcnt(" #n ")" ::: "memory")
#define SB0() __builtin_amdgcn_sched_barrier(0)
#define WAITL0() do { asm volatile("" ::: "memory"); SB0(); __builtin_amdgcn_s_waitcnt(0xC07F); SB0(); asm volatile("" ::: "memory"); } while (0)
#define PAUX 16
#define LPRIO 0
#define KVAUX 2

constexpr int LDS_X = 0;
constexpr int LDS_K = 32768;
constexpr int LDS_W = 65536;
constexpr int LDS_V = 131072;
constexpr int LDS_TOTAL = 163840;

template <int AUX>
DI void dma16(const char* g, LDSP char* l) {
  __builtin_amdgcn_global_load_lds((const GASP unsigned*)g, (LDSP unsigned*)l, 16, 0, AUX);
}
DI bf16x8 tr_frag(LDSP char* p, int off_lo, int off_hi) {
  bf16x4 a = __builtin_amdgcn_ds_read_tr16_b64_v4bf16((LDSP bf16x4*)(p + off_lo));
  bf16x4 b = __builtin_amdgcn_ds_read_tr16_b64_v4bf16((LDSP bf16x4*)(p + off_hi));
  return __builtin_shufflevector(a, b, 0, 1, 2, 3, 4, 5, 6, 7);
}
DI bf16x8 pack8(const f32x16& x, int s) {
  bf16x8 p;
#pragma unroll
  for (int j = 0; j < 8; ++j) p[j] = (__bf16)x[8 * s + j];
  return p;
}

#define TS()
#define TSREC()
#define TSDUMP()
#define CBAR() BAR()
#define CWAITL0() WAITL0()
#define STAMP(k)
struct FragSet { bf16x8 a[2][4]; bf16x8 b[2]; };

__global__ __launch_bounds__(512, 2) void fused_kernel(const char* __restrict__ x, const char* __restrict__ kc,
                                                       const char* __restrict__ vc, const char* __restrict__ wq,
                                                       const char* __restrict__ wo, char* __restrict__ part) {
  __shared__ __attribute__((aligned(16))) char smem_[LDS_TOTAL];
  LDSP char* smem = (LDSP char*)smem_;
  const int wid = __builtin_amdgcn_readfirstlane(threadIdx.x >> 6);
  const int lane = threadIdx.x & 63;
  const int bid = blockIdx.x;
  const int hp = (bid & 7) * 2 + ((bid >> 3) & 1);
  const int b = bid >> 4;
  const int u0 = ((b >> 1) + 4 * (hp & 1)) & 7;
  const int swp = b & 1;

  if (wid >= 4) {
    const int lw = wid - 4;
    const int hi = lane >> 5;
    const auto rq = __builtin_amdgcn_make_buffer_rsrc((void*)wq, 0, 4 << 20, 0x00020000);
    const auto ro = __builtin_amdgcn_make_buffer_rsrc((void*)wo, 0, 4 << 20, 0x00020000);
    const auto rx = __builtin_amdgcn_make_buffer_rsrc((void*)x, 0, 1 << 20, 0x00020000);
    const auto rk = __builtin_amdgcn_make_buffer_rsrc((void*)kc, 0, 8 << 20, 0x00020000);
    const auto rv = __builtin_amdgcn_make_buffer_rsrc((void*)vc, 0, 8 << 20, 0x00020000);
    const int wq_e = (8 * lw + hi) * 8192 + hp * 512 + (((lane & 31) ^ (hi << 2)) << 4);
    const int wq_o = (8 * lw + 2 + hi) * 8192 + hp * 512 + (((lane & 31) ^ ((2 + hi) << 2)) << 4);
    const int xg = b * 65536 + (8 * lw + (lane >> 3)) * 1024 + (((lane & 7) ^ (4 * (lw & 1) + (lane >> 4))) << 4);
    const int c16 = ((lane & 15) ^ ((lane >> 4) << 2)) << 4;
    const int wog = (hp * 256 + 16 * lw + (lane >> 4)) * 1024 + c16;
    const int kvb = (b * 32 + hp * 2) * 16384;
    const int vg = kvb + (16 * lw + (lane >> 4)) * 256 + c16;
    const int kg = kvb + (4 * lw + (lane >> 4)) * 256 + (((lane & 15) ^ (4 * lw + (lane >> 4))) << 4);
    const int wopf = (hp * 256 + b * 16 + lw * 4) * 1024 + lane * 64;
#define DMA(rs, vo, so, l, aux) __builtin_amdgcn_raw_ptr_buffer_load_lds(rs, (LDSP unsigned*)(l), 16, vo, so, 0, aux)
    __builtin_amdgcn_s_setprio(LPRIO);

    auto issue_wq = [&](int i) {
      const int ks = 2 * ((u0 + (i >> 1)) & 7) + ((i & 1) ^ swp);
      LDSP char* d = smem + LDS_W + (i & 3) * 16384 + lw * 4096;
      const int go = ks * 32 * 8192;
      DMA(rq, wq_e, go, d, 0);
      DMA(rq, wq_o, go, d + 1024, 0);
      DMA(rq, wq_e, go + 4 * 8192, d + 2048, 0);
      DMA(rq, wq_o, go + 4 * 8192, d + 3072, 0);
    };
    auto issue_x = [&](int u, int hf) {
      DMA(rx, xg, hf * 32768 + ((u0 + u) & 7) * 128, smem + LDS_X + (u & 3) * 8192 + hf * 4096 + lw * 1024, 0);
    };
    auto issue_wo = [&](int t) {
      LDSP char* d = smem + (t >= 8 ? t - 8 : t + 2) * 16384 + lw * 4096;
      const int go = (t & 3) * 64 * 1024 + (t >> 2) * 256;
      DMA(ro, wog, go, d, 0);
      DMA(ro, wog, go + 4096, d + 1024, 0);
      DMA(ro, wog, go + 8192, d + 2048, 0);
      DMA(ro, wog, go + 12288, d + 3072, 0);
    };
    auto issue_k = [&](int hd) {
#pragma unroll
      for (int i = 0; i < 4; ++i) DMA(rk, kg, hd * 16384 + i * 4096, smem + LDS_K + hd * 16384 + i * 4096 + lw * 1024, KVAUX);
    };
    auto issue_v = [&](int hd) {
#pragma unroll
      for (int i = 0; i < 4; ++i) DMA(rv, vg, hd * 16384 + i * 1024, smem + LDS_V + hd * 16384 + lw * 4096 + i * 1024, KVAUX);
    };
    issue_wq(0);
#pragma unroll
    for (int u = 0; u < 4; ++u) { issue_x(u, 0); issue_x(u, 1); }
    issue_k(0);
    int lb[3];
    lb[0] = lw * 4096 + lane * 16; lb[1] = lb[0] + 65536; lb[2] = lb[0] + 131072;
    asm volatile("" : "+v"(lb[0]), "+v"(lb[1]), "+v"(lb[2]));
    WAITV(8); BAR();
    issue_wq(1); issue_k(1); issue_wq(2);
    WAITV(8); BAR();
    issue_v(0); issue_wq(3);
    WAITV(8); BAR();
    issue_v(1); issue_wq(4);
    WAITV(8); BAR();
    issue_wq(5); issue_x(4, 0);
#pragma unroll 1
    for (int i = 6; i <= 12; ++i) {
      issue_wq(i); issue_x((i + 3) >> 1, (i + 1) & 1);
      WAITV(10); BAR();
    }
    issue_wq(13); WAITV(9); BAR();
    issue_wq(14); WAITV(8); BAR();
    issue_wq(15);
    __builtin_amdgcn_raw_ptr_buffer_load_lds(ro, (LDSP unsigned*)(smem + LDS_K + lw * 4096), 4, wopf, 0, 0, 0);
    issue_wo(0);
    WAITV(13); BAR();
    WAITV(9); BAR();
    WAITV(5); BAR();
    issue_wo(1);
    BAR();
#pragma unroll 1
    for (int t = 2; t < 6; ++t) issue_wo(t);
    u32x4 st[10][4];
#define GET(t) do { _Pragma("unroll") for (int i = 0; i < 4; ++i) \
        asm volatile("buffer_load_dwordx4 %0, %1, %2, %3 offen" : "=v"(st[(t) - 6][i]) : "v"(wog), "s"(ro), "s"(((t) & 3) * 64 * 1024 + ((t) >> 2) * 256 + i * 4096) : "memory"); } while (0)
#define PUT(t, n) do { asm volatile("s_waitcnt vmcnt(" #n ")" : "+v"(st[(t) - 6][0]), "+v"(st[(t) - 6][1]), "+v"(st[(t) - 6][2]), "+v"(st[(t) - 6][3]) :: "memory"); \
      _Pragma("unroll") for (int i = 0; i < 4; ++i) { constexpr int sl = ((t) >= 8 ? (t) - 8 : (t) + 2); \
        *(LDSP u32x4*)(smem + lb[sl >> 2] + (sl & 3) * 16384 + i * 1024) = st[(t) - 6][i]; } } while (0)
    GET(6); GET(7); GET(8); GET(9); GET(10);
    WAITV(20); BAR();
    GET(11); GET(12); GET(13);
    PUT(6, 28); PUT(7, 24);
    WAITL0(); BAR();
    GET(14); GET(15);
    PUT(8, 28); PUT(9, 24); PUT(10, 20); PUT(11, 16);
    WAITL0(); BAR();
    PUT(12, 12); PUT(13, 8); PUT(14, 4); PUT(15, 0);
    WAITL0(); BAR();
    return;
  }

  const int h = wid >> 1, qh = wid & 1;
  const int r = lane & 31, hh = lane >> 5;
  const int blk = (lane >> 4) & 1, q4 = (lane >> 2) & 3, p4 = lane & 3;
  const int pp = ((p4 & 1) << 1) | (p4 >> 1);
  const int pir = (r & 19) | ((r & 4) << 1) | ((r & 8) >> 1);
  STAMP(0);

  int woff[4];
#pragma unroll
  for (int dt = 0; dt < 4; ++dt) woff[dt] = LDS_W + (8 * hh + q4) * 512 + h * 256 + ((dt ^ q4) << 6) + blk * 32 + pp * 8;
  const int xrow = 32 * qh + r;
  int xoff[4];
#pragma unroll
  for (int ks = 0; ks < 4; ++ks) xoff[ks] = LDS_X + xrow * 128 + (((2 * (ks ^ (2 * swp)) + hh) ^ ((xrow >> 1) & 7)) << 4);

  int voff[4];
#pragma unroll
  for (int dt = 0; dt < 4; ++dt) voff[dt] = LDS_V + h * 16384 + (8 * hh + q4) * 256 + ((dt ^ q4) << 6) + blk * 32 + pp * 8;
  int ooff = LDS_X + (qh * 16 + h * 8) * 1024 + lane * 16;
  asm volatile("" : "+v"(voff[0]), "+v"(voff[1]), "+v"(voff[2]), "+v"(voff[3]), "+v"(ooff));
  f32x16 accq[4];
#pragma unroll
  for (int dt = 0; dt < 4; ++dt)
#pragma unroll
    for (int i = 0; i < 16; ++i) accq[dt][i] = 0.f;

  auto read_stage = [&](FragSet& f, int slot, int xu, int par) {
    LDSP char* wb = smem + slot * 16384;
    LDSP char* xb = smem + xu * 8192;
#pragma unroll
    for (int ks = 0; ks < 2; ++ks) {
      f.b[ks] = *(LDSP bf16x8*)(xb + xoff[2 * par + ks]);
#pragma unroll
      for (int dt = 0; dt < 4; ++dt) f.a[ks][dt] = tr_frag(wb + woff[dt], ks * 8192, ks * 8192 + 2048);
    }
  };
  auto mma_stage = [&](const FragSet& f) {
#pragma unroll
    for (int ks = 0; ks < 2; ++ks)
#pragma unroll
      for (int dt = 0; dt < 4; ++dt) accq[dt] = MFMA(f.a[ks][dt], f.b[ks], accq[dt]);
  };
#define INTERLEAVE(NM, ND) do { _Pragma("unroll") for (int z = 0; z < NM; ++z) { \
    __builtin_amdgcn_sched_group_barrier(0x8, 1, 0); __builtin_amdgcn_sched_group_barrier(0x100, ND, 0); } } while (0)

  bf16x8 kf[2][8];
  FragSet f0, f1;
  CBAR();
  STAMP(1);
  read_stage(f0, 0, 0, 0);
  CWAITL0();
#pragma unroll 1
  for (int u = 0; u < 7; ++u) {
    CBAR();
    read_stage(f1, (2 * u + 1) & 3, u & 3, 1);
    mma_stage(f0);
    INTERLEAVE(8, 3);
    CWAITL0();
    CBAR();
    read_stage(f0, (2 * u + 2) & 3, (u + 1) & 3, 0);
    mma_stage(f1);
    INTERLEAVE(8, 3);
    CWAITL0();
    if (u == 4) {
      LDSP char* kb = smem + LDS_K + h * 16384 + pir * 256;
#pragma unroll
      for (int kt = 0; kt < 2; ++kt)
#pragma unroll
        for (int s = 0; s < 8; ++s) kf[kt][s] = *(LDSP bf16x8*)(kb + kt * 8192 + (((2 * s + hh) ^ (pir & 15)) << 4));
    }
  }
  CBAR();
  read_stage(f1, 3, 3, 1);
  mma_stage(f0);
  INTERLEAVE(8, 3);
  CWAITL0();
  CBAR();
  bf16x8 qf[8], vf[4][4], pf[4];
  f32x16 accs[2], acco[4];
  const f32x16 zero16 = {0.f, 0.f, 0.f, 0.f, 0.f, 0.f, 0.f, 0.f, 0.f, 0.f, 0.f, 0.f, 0.f, 0.f, 0.f, 0.f};
#define QM(dt, ks) accq[dt] = MFMA(f1.a[ks][dt], f1.b[ks], accq[dt])
#define QP(i) qf[i] = pack8(accq[(i) >> 1], (i) & 1)
#define SM(kt, s) accs[kt] = MFMA(kf[kt][s], qf[s], (s) == 0 ? zero16 : accs[kt])
#define VR(dt, s) vf[dt][s] = tr_frag(smem + voff[dt], (s) * 4096, (s) * 4096 + 1024)
  QM(0, 0); QM(0, 1); QM(1, 0); QM(1, 1); SB0();
  QP(0); SB0(); QM(2, 0); SB0();
  QP(1); SB0(); QM(2, 1); SB0();
  QP(2); SB0(); QM(3, 0); SB0();
  QP(3); SB0(); QM(3, 1); SB0();
  VR(0, 0); VR(1, 0); SB0(); SM(0, 0); SB0();
  QP(4); SB0(); SM(0, 1); SB0();
  QP(5); SB0(); SM(0, 2); SB0();
  QP(6); SB0(); SM(0, 3); SB0();
  QP(7); SB0(); SM(0, 4); SB0();
  VR(2, 0); VR(3, 0); SB0(); SM(0, 5); SB0();
  VR(0, 1); VR(1, 1); SB0(); SM(0, 6); SB0();
  VR(2, 1); VR(3, 1); SB0(); SM(0, 7); SB0();
  VR(0, 2); VR(0, 3); SB0(); SM(1, 0); SB0();
  VR(1, 2); VR(1, 3); SB0(); SM(1, 1); SB0();
  VR(2, 2); VR(2, 3); SB0(); SM(1, 2); SB0();
  VR(3, 2); VR(3, 3); SB0(); SM(1, 3); SB0();
  float mx = fmaxf(fmaxf(accs[0][0], accs[0][1]), accs[0][2]);
  mx = fmaxf(fmaxf(mx, accs[0][3]), accs[0][4]); mx = fmaxf(fmaxf(mx, accs[0][5]), accs[0][6]);
  SB0(); SM(1, 4); SB0();
  mx = fmaxf(fmaxf(mx, accs[0][7]), accs[0][8]); mx = fmaxf(fmaxf(mx, accs[0][9]), accs[0][10]); mx = fmaxf(fmaxf(mx, accs[0][11]), accs[0][12]);
  SB0(); SM(1, 5); SB0();
  mx = fmaxf(fmaxf(mx, accs[0][13]), accs[0][14]); mx = fmaxf(mx, accs[0][15]);
  SB0(); SM(1, 6); SM(1, 7); SB0();
  STAMP(2);
  {
    float m2 = fmaxf(fmaxf(accs[1][0], accs[1][1]), accs[1][2]);
#pragma unroll
    for (int i = 3; i < 9; i += 2) m2 = fmaxf(fmaxf(m2, accs[1][i]), accs[1][i + 1]);
#pragma unroll
    for (int i = 9; i < 15; i += 2) mx = fmaxf(fmaxf(mx, accs[1][i]), accs[1][i + 1]);
    mx = fmaxf(fmaxf(mx, m2), accs[1][15]);
    auto sw = __builtin_amdgcn_permlane32_swap(__float_as_uint(mx), __float_as_uint(mx), false, false);
    mx = fmaxf(__uint_as_float(sw[0]), __uint_as_float(sw[1]));
  }
  const float sc = 0.08838834764831845f * 1.4426950408889634f;
  const float mb = mx * sc;
  f32x2 sum2 = {0.f, 0.f}, sum3 = {0.f, 0.f};
#pragma unroll
  for (int kt = 0; kt < 2; ++kt)
#pragma unroll
    for (int i = 0; i < 16; i += 2) {
      f32x2 t = {accs[kt][i], accs[kt][i + 1]};
      t = t * sc - mb;
      t[0] = __builtin_amdgcn_exp2f(t[0]); t[1] = __builtin_amdgcn_exp2f(t[1]);
      accs[kt][i] = t[0]; accs[kt][i + 1] = t[1];
      if (i & 2) sum3 += t; else sum2 += t;
    }
  sum2 += sum3;
  float sum = sum2[0] + sum2[1];
  {
    auto sw = __builtin_amdgcn_permlane32_swap(__float_as_uint(sum), __float_as_uint(sum), false, false);
    sum = __uint_as_float(sw[0]) + __uint_as_float(sw[1]);
  }
  const float inv = __builtin_amdgcn_rcpf(sum);
  SB0();
#define PH(s, hf) do { _Pragma("unroll") for (int j = 4 * (hf); j < 4 * (hf) + 4; j += 2) { \
    f32x2 t = {accs[(s) >> 1][8 * ((s) & 1) + j], accs[(s) >> 1][8 * ((s) & 1) + j + 1]}; t *= inv; \
    pf[s][j] = (__bf16)t[0]; pf[s][j + 1] = (__bf16)t[1]; } } while (0)
#define OM(dt, s) acco[dt] = MFMA(vf[dt][s], pf[s], (s) == 0 ? zero16 : acco[dt])
#define OW(i) *(LDSP bf16x8*)(smem + ooff + (i) * 1024) = pack8(acco[(i) >> 1], (i) & 1)
  PH(0, 0); PH(0, 1); SB0();
  OM(0, 0); SB0(); PH(1, 0); SB0();
  OM(1, 0); SB0(); PH(1, 1); SB0();
  OM(2, 0); SB0(); PH(2, 0); SB0();
  OM(3, 0); SB0(); PH(2, 1); SB0();
  OM(0, 1); SB0(); PH(3, 0); SB0();
  OM(1, 1); SB0(); PH(3, 1); SB0();
  OM(2, 1); OM(3, 1); OM(0, 2); OM(0, 3); OM(1, 2); OM(1, 3); SB0();
  OW(0); SB0(); OM(2, 2); SB0();
  OW(1); SB0(); OM(2, 3); SB0();
  OW(2); SB0(); OM(3, 2); SB0();
  OW(3); SB0(); OM(3, 3); SB0();
  OW(4); OW(5); SB0(); OW(6); OW(7);
  CWAITL0();
  STAMP(3);
  CBAR();
  STAMP(4);

  int wob[3];
  wob[0] = (8 * hh + q4) * 256 + ((wid ^ q4) << 6) + blk * 32 + pp * 8; wob[1] = wob[0] + 65536; wob[2] = wob[0] + 131072;
  asm volatile("" : "+v"(wob[0]), "+v"(wob[1]), "+v"(wob[2]));
  bf16x8 wf[2][4];
  bf16x8 of[2][16];
  f32x16 acc[2][2];
  const auto prs = __builtin_amdgcn_make_buffer_rsrc(part, 0, 16 << 20, 0x00020000);
  const int pofs = ((b * 4 * 16 + 4 * wid + (r >> 4)) * 16 + hp) * 1024 + hh * 256 + (r & 15) * 16;
  auto read_wo = [&](int t) {
    const int sl = (t >= 8 ? t - 8 : t + 2);
    LDSP char* p = smem + wob[sl >> 2] + (sl & 3) * 16384;
#pragma unroll
    for (int ks = 0; ks < 4; ++ks) wf[t & 1][ks] = tr_frag(p, ks * 4096, ks * 4096 + 1024);
  };
  auto read_of = [&](int tq) {
#pragma unroll
    for (int ks = 4 * tq; ks < 4 * tq + 4; ++ks)
#pragma unroll
      for (int qt = 0; qt < 2; ++qt) of[qt][ks] = *(LDSP bf16x8*)(smem + LDS_X + (qt * 16 + ks) * 1024 + lane * 16);
  };
  auto store_chunk = [&](int c) {
#pragma unroll
    for (int qt = 0; qt < 2; ++qt)
#pragma unroll
      for (int a = 0; a < 2; ++a)
        __builtin_amdgcn_raw_buffer_store_b128(__builtin_bit_cast(u32x4, pack8(acc[c & 1][qt], a)), prs,
                                               pofs + (c * 16 + 2 * qt) * 16384 + a * 512, 0, PAUX);
  };
  read_wo(0); read_of(0);
  SB0();
#pragma unroll
  for (int t = 0; t < 16; ++t) {
    const int c = t >> 2, tq = t & 3;
    if (t == 2 || t == 6 || t == 11) { CWAITL0(); CBAR(); }
    if (t < 15) read_wo(t + 1);
    if (t == 0) { read_of(1); read_of(2); }
    if (t == 1) read_of(3);
    if (tq == 0) { acc[c & 1][0] = zero16; acc[c & 1][1] = zero16; }
#pragma unroll
    for (int ks = 0; ks < 4; ++ks)
#pragma unroll
      for (int qt = 0; qt < 2; ++qt) acc[c & 1][qt] = MFMA(wf[t & 1][ks], of[qt][4 * tq + ks], acc[c & 1][qt]);
    if (tq == 0 && c > 0) store_chunk(c - 1);
    if (t == 0) { INTERLEAVE(8, 3); }
    else if (t == 1) { INTERLEAVE(8, 2); }
    else if (tq == 0) {
      INTERLEAVE(4, 2);
#pragma unroll
      for (int z = 0; z < 4; ++z) { __builtin_amdgcn_sched_group_barrier(0x8, 1, 0); __builtin_amdgcn_sched_group_barrier(0x2, 5, 0); __builtin_amdgcn_sched_group_barrier(0x40, 1, 0); }
    } else { INTERLEAVE(4, 2); }
    SB0();
    if (tq == 3) STAMP(5 + c);
  }
  store_chunk(3);
  TS(); CWAITL0(); WAITV(0); TS(); CWAITL0(); TSDUMP();
}

__global__ __launch_bounds__(256) void reduce_kernel(char* __restrict__ part, char* __restrict__ out) {
  const int bid = blockIdx.x, lane = threadIdx.x & 63, c = threadIdx.x >> 6;
  const int b = bid >> 4, j = bid & 15;
  const char* p = part + (size_t)((b * 4 + c) * 16 + j) * 16384 + lane * 16;
  u32x4 v[16];
#pragma unroll
  for (int i = 0; i < 16; ++i) v[i] = *(const u32x4*)(p + i * 1024);
  float s[8];
#pragma unroll
  for (int e = 0; e < 8; ++e) s[e] = 0.f;
#pragma unroll
  for (int i = 0; i < 16; ++i)
#pragma unroll
    for (int e = 0; e < 4; ++e) {
      s[2 * e] += __uint_as_float(v[i][e] << 16);
      s[2 * e + 1] += __uint_as_float(v[i][e] & 0xffff0000u);
    }
  bf16x8 o;
#pragma unroll
  for (int e = 0; e < 8; ++e) o[e] = (__bf16)s[e];
  const int w = j >> 2, qt = (j >> 1) & 1, hf = j & 1;
  const int row = b * 64 + 32 * qt + 16 * hf + (lane & 15);
  const int col = 128 * c + 32 * w + 16 * (lane >> 5) + 8 * ((lane >> 4) & 1);
  *(bf16x8*)(out + (size_t)row * 1024 + col * 2) = o;
}

extern "C" void kernel_launch(void* const* d_in, const int* in_sizes, int n_in, void* d_out, int out_size, void* d_ws,
                              size_t ws_size, hipStream_t stream) {
  fused_kernel<<<256, 512, 0, stream>>>((const char*)d_in[0], (const char*)d_in[1], (const char*)d_in[2],
                                        (const char*)d_in[3], (const char*)d_in[4], (char*)d_ws);
  reduce_kernel<<<256, 256, 0, stream>>>((char*)d_ws, (char*)d_out);
}
```

```cpp
#include <hip/hip_runtime.h>
#include <hip/hip_bf16.h>

typedef __bf16 bf16x4 __attribute__((ext_vector_type(4)));
typedef __bf16 bf16x8 __attribute__((ext_vector_type(8)));
typedef float f32x16 __attribute__((ext_vector_type(16)));
typedef float f32x2 __attribute__((ext_vector_type(2)));
typedef unsigned u32x4 __attribute__((ext_vector_type(4)));
#define LDSP __attribute__((address_space(3)))
#define DI __device__ __forceinline__
#define MFMA(a, b, c) __builtin_amdgcn_mfma_f32_32x32x16_bf16((a), (b), (c), 0, 0, 0)
#define BAR() do { asm volatile("" ::: "memory"); __builtin_amdgcn_s_barrier(); asm volatile("" ::: "memory"); } while (0)
#define WAITV(n) asm volatile("s_waitcnt vmcnt(" #n ")" ::: "memory")
#define SB0() __builtin_amdgcn_sched_barrier(0)
#define WAITL0() do { asm volatile("" ::: "memory"); SB0(); __builtin_amdgcn_s_waitcnt(0xC07F); SB0(); asm volatile("" ::: "memory"); } while (0)
#define PAUX 16
#define WOEARLY 1
#define RAUX 16
#define FL7 0
#define KVAUX 2

constexpr int LDS_X = 0;
constexpr int LDS_K = 32768;
constexpr int LDS_W = 65536;
constexpr int LDS_V = 131072;
constexpr int LDS_TOTAL = 163840;
constexpr int WS_F = 16 << 20;
constexpr int WS_A = WS_F + (256 << 10);
constexpr int WS_BYTES = WS_A + 4096;
constexpr unsigned OOB = 0x7ffffff0u;
constexpr int SPIN_CAP = 1 << 16;
#define PDELAY 8
#define EARLYREL 0
#define PSLEEP 4
#define PBLK(pb) ((0x13204132u >> (4 * (pb))) & 15)

DI bf16x8 tr_frag(LDSP char* p, int off_lo, int off_hi) {
  bf16x4 a = __builtin_amdgcn_ds_read_tr16_b64_v4bf16((LDSP bf16x4*)(p + off_lo));
  bf16x4 b = __builtin_amdgcn_ds_read_tr16_b64_v4bf16((LDSP bf16x4*)(p + off_hi));
  return __builtin_shufflevector(a, b, 0, 1, 2, 3, 4, 5, 6, 7);
}
DI unsigned ld_sc1(__amdgpu_buffer_rsrc_t rs, unsigned ofs) {
  unsigned v;
  asm volatile("buffer_load_dword %0, %1, %2, 0 offen sc1\n\ts_waitcnt vmcnt(0)" : "=&v"(v) : "v"(ofs), "s"(rs) : "memory");
  return v;
}
DI bf16x8 pack8(const f32x16& x, int s) {
  bf16x8 p;
#pragma unroll
  for (int j = 0; j < 8; ++j) p[j] = (__bf16)x[8 * s + j];
  return p;
}

#define TS()
#define TSREC()
#define TSDUMP()
#define CBAR() BAR()
#define LT(k)
#define STAMP(k)
struct FragSet { bf16x8 a[2][4]; bf16x8 b[2]; };

__global__ __launch_bounds__(768, 3) void fused_kernel(const char* __restrict__ x, const char* __restrict__ kc,
                                                       const char* __restrict__ vc, const char* __restrict__ wq,
                                                       const char* __restrict__ wo, char* part, char* out) {
  __shared__ __attribute__((aligned(16))) char smem_[LDS_TOTAL];
  LDSP char* smem = (LDSP char*)smem_;
  const int wid = __builtin_amdgcn_readfirstlane(threadIdx.x >> 6);
  const int lane = threadIdx.x & 63;
  const int bid = blockIdx.x;
  const int hp = (bid & 7) * 2 + ((bid >> 3) & 1);
  const int b = bid >> 4;
  const int grpB = b & 1;
  const int u0 = b >> 1;
  const int swp = b & 1;

  if (wid >= 4) {
    const int lw = wid - 4;
    LT(0);
    const int hi = lane >> 5;
    const auto rq = __builtin_amdgcn_make_buffer_rsrc((void*)wq, 0, 4 << 20, 0x00020000);
    const auto ro = __builtin_amdgcn_make_buffer_rsrc((void*)wo, 0, 4 << 20, 0x00020000);
    const auto rx = __builtin_amdgcn_make_buffer_rsrc((void*)x, 0, 1 << 20, 0x00020000);
    const auto rk = __builtin_amdgcn_make_buffer_rsrc((void*)kc, 0, 8 << 20, 0x00020000);
    const auto rv = __builtin_amdgcn_make_buffer_rsrc((void*)vc, 0, 8 << 20, 0x00020000);
    const int wq_e = (4 * lw + hi) * 8192 + hp * 512 + (((lane & 31) ^ (hi << 2)) << 4);
    const int wq_o = (4 * lw + 2 + hi) * 8192 + hp * 512 + (((lane & 31) ^ ((2 + hi) << 2)) << 4);
    const int xg = b * 65536 + (8 * lw + (lane >> 3)) * 1024 + (((lane & 7) ^ (4 * (lw & 1) + (lane >> 4))) << 4);
#define DMA(rs, vo, so, l, aux) __builtin_amdgcn_raw_ptr_buffer_load_lds(rs, (LDSP unsigned*)(l), 16, vo, so, 0, aux)
#define DMA4(rs, vo, so, l) __builtin_amdgcn_raw_ptr_buffer_load_lds(rs, (LDSP unsigned*)(l), 4, vo, so, 0, 0)
    auto issue_pair = [&](int j, int blkofs) {
      const int ku = (u0 + j) & 7;
      LDSP char* d = smem + blkofs + lw * 2048;
#pragma unroll
      for (int sg = 0; sg < 2; ++sg) {
        const int go = (2 * ku + (sg ^ swp)) * 32 * 8192;
        DMA(rq, wq_e, go, d + sg * 16384, 0);
        DMA(rq, wq_o, go, d + sg * 16384 + 1024, 0);
        if (sg == 0) DMA(rx, xg, ku * 128, smem + LDS_X + (j & 3) * 8192 + lw * 1024, 0);
      }
    };
    issue_pair(0, LDS_W);
    const int c16 = ((lane & 15) ^ ((lane >> 4) << 2)) << 4;
    const int wog = (hp * 256 + 32 * lw + (lane >> 3)) * 1024 + (((lane & 7) ^ (((lane >> 4) & 1) << 2)) << 4);
    const int kvb = (b * 32 + hp * 2) * 16384;
    const int vg = kvb + (16 * lw + (lane >> 4)) * 256 + c16;
    const int kg = kvb + (4 * lw + (lane >> 4)) * 256 + (((lane & 15) ^ (4 * (lw & 3) + (lane >> 4))) << 4);
    auto issue_k = [&]() {
#pragma unroll
      for (int i = 0; i < 4; ++i) DMA(rk, kg, i * 8192, smem + LDS_K + i * 8192 + lw * 1024, KVAUX);
    };
    auto issue_v = [&]() {
#pragma unroll
      for (int i = 0; i < 4; ++i) DMA(rv, vg, i * 1024, smem + LDS_V + lw * 4096 + i * 1024, KVAUX);
    };
    auto issue_wo = [&](int c) {
      LDSP char* d = smem + PBLK(c) * 32768 + lw * 4096;
#pragma unroll
      for (int i = 0; i < 4; ++i) DMA(ro, wog, c * 128 + i * 8192, d + i * 1024, 0);
    };
    int pfo = grpB ? (hp * 256 + (b >> 1) * 32 + lw * 4) * 1024 + lane * 64
                   : b * 65536 + ((hp & 1) * 32 + lw * 4) * 1024 + lane * 64;
    asm volatile("" : "+v"(pfo));
    if (!grpB) {
      DMA4(rx, pfo, 65536, smem + LDS_K + lw * 1024);
      DMA4(rx, pfo, 0, smem + LDS_K + lw * 1024);
      issue_k();
    }
    issue_pair(1, LDS_W + 32768);
    WAITV(5); BAR();
    unsigned nonce;
    asm volatile("ds_read_b32 %0, %1\n\ts_waitcnt lgkmcnt(0)" : "=v"(nonce) : "v"(LDS_X + 24576) : "memory");
    {
      int bo = LDS_W + 65536;
#pragma unroll 1
      for (int j = 2; j < 8; ++j) {
        issue_pair(j, bo);
        bo = (bo == LDS_W + 65536) ? LDS_W : bo + 32768;
        WAITV(5); BAR();
      }
    }
    LT(1);
    if (grpB) {
      DMA4(ro, pfo, 0, smem + LDS_K + lw * 1024);
      WAITV(1); BAR();
      issue_k();
      BAR();
      issue_v();
      WAITV(4); BAR();
    } else {
      issue_v();
      WAITV(4); BAR();
      BAR();
      BAR();
    }
    LT(2);
    issue_wo(0);
    LT(3); WAITV(4); LT(4); BAR(); LT(5);
    issue_wo(1);
    issue_wo(2);
    LT(6); BAR(); LT(7);
    LT(8); WAITV(8); LT(9); BAR(); LT(10);
    issue_wo(3);
    LT(11); WAITV(8); BAR(); LT(12);
    issue_wo(4);
    LT(13); WAITV(8); BAR(); LT(14);
    issue_wo(5);
    LT(15); WAITV(8); BAR(); LT(16);
    issue_wo(6);
    LT(17); WAITV(8); BAR(); LT(18);
    issue_wo(7);
    if (lw == 7) *(volatile LDSP int*)(smem + LDS_V) = 0;
    LT(19); WAITV(8); BAR(); LT(20);
    LT(21); WAITV(0); WAITL0(); BAR(); LT(22);
    LT(23);
    {
      const auto prs = __builtin_amdgcn_make_buffer_rsrc(part, 0, WS_BYTES, 0x00020000);
      const auto rout = __builtin_amdgcn_make_buffer_rsrc(out, 0, 1 << 20, 0x00020000);
      const int fofs = WS_F + ((b * 16 + (lane & 15)) * 4 + (hp >> 2)) * 256 + (hp & 3) * 4;
      const int row = b * 64 + 32 * ((hp >> 2) & 1) + 8 * (hp & 3) + (lane & 7);
      const unsigned oofs = lane < 32 ? row * 1024 + (32 * (hp >> 3) + 16 * ((lane >> 4) & 1) + 8 * ((lane >> 3) & 1)) * 2 : OOB;
      const int c = lw;
      if (lw == 7) {
        int cnt = lane < 16 ? 0 : 8, pub = 0;
        __builtin_amdgcn_s_sleep(PDELAY);
#pragma unroll 1
        for (int spin = 0; spin < SPIN_CAP; ++spin) {
          const unsigned f = ld_sc1(prs, cnt > pub ? OOB : fofs);
          if (cnt <= pub && (f >> 4) == nonce) cnt = f & 15;
          const int mn = __all(cnt >= 8) ? 8 : (__all(cnt >= 6) ? 6 : (__all(cnt >= 4) ? 4 : (__all(cnt >= 2) ? 2 : 0)));
          if (mn > pub) { pub = mn; *(volatile LDSP int*)(smem + LDS_V) = mn; if (mn == 8) break; }
          else __builtin_amdgcn_s_sleep(PSLEEP);
        }
        *(volatile LDSP int*)(smem + LDS_V) = 8;
      } else {
#pragma unroll 1
        for (int spin = 0; spin < (SPIN_CAP << 6); ++spin) {
          if (__builtin_amdgcn_readfirstlane(*(volatile LDSP int*)(smem + LDS_V)) > c) break;
          __builtin_amdgcn_s_sleep(1);
        }
      }
      asm volatile("" ::: "memory");
      u32x4 v[8];
#pragma unroll
      for (int i = 0; i < 8; ++i) v[i] = __builtin_amdgcn_raw_buffer_load_b128(prs, lane * 16 + (i & 3) * 1024, ((b * 8 + c) * 16 + hp) * 8192 + (i >> 2) * 4096, RAUX);
      SB0();
      float sm[8];
#pragma unroll
      for (int e = 0; e < 4; ++e) { sm[2 * e] = __uint_as_float(v[0][e] << 16); sm[2 * e + 1] = __uint_as_float(v[0][e] & 0xffff0000u); }
#pragma unroll
      for (int i = 1; i < 8; ++i)
#pragma unroll
        for (int e = 0; e < 4; ++e) { sm[2 * e] += __uint_as_float(v[i][e] << 16); sm[2 * e + 1] += __uint_as_float(v[i][e] & 0xffff0000u); }
      bf16x8 o;
#pragma unroll
      for (int e = 0; e < 8; ++e) {
        auto sw = __builtin_amdgcn_permlane32_swap(__float_as_uint(sm[e]), __float_as_uint(sm[e]), false, false);
        o[e] = (__bf16)(__uint_as_float(sw[0]) + __uint_as_float(sw[1]));
      }
      __builtin_amdgcn_raw_buffer_store_b128(__builtin_bit_cast(u32x4, o), rout, oofs + c * 128, 0, 0);
    }
    return;
  }

  const int h = wid >> 1, qh = wid & 1;
  const int r = lane & 31, hh = lane >> 5;
  const int blk = (lane >> 4) & 1, q4 = (lane >> 2) & 3, p4 = lane & 3;
  const int pp = ((p4 & 1) << 1) | (p4 >> 1);
  const int pir = (r & 19) | ((r & 4) << 1) | ((r & 8) >> 1);
  STAMP(0);
  const auto prs = __builtin_amdgcn_make_buffer_rsrc(part, 0, WS_BYTES, 0x00020000);
  if (wid == 0) {
    const unsigned f = ld_sc1(prs, WS_F + ((b * 16 + (lane & 15)) * 4 + (hp >> 2)) * 256 + (hp & 3) * 4) >> 4;
    unsigned cand = 0;
#pragma unroll 1
    while (__any(f == cand)) ++cand;
    __builtin_amdgcn_raw_buffer_store_b32(cand, prs, lane == 0 ? WS_A + b * 256 + hp * 4 : OOB, 0, 16);
    *(LDSP unsigned*)(smem + LDS_X + 24576) = cand;
    WAITV(0);
    WAITL0();
  }

  int woff[4];
#pragma unroll
  for (int dt = 0; dt < 4; ++dt) woff[dt] = LDS_W + (8 * hh + q4) * 512 + h * 256 + ((dt ^ q4) << 6) + blk * 32 + pp * 8;
  const int xrow = 32 * qh + r;
  int xoff[4];
#pragma unroll
  for (int ks = 0; ks < 4; ++ks) xoff[ks] = LDS_X + xrow * 128 + (((2 * (ks ^ (2 * swp)) + hh) ^ ((xrow >> 1) & 7)) << 4);
  int voff[4], koff[8], wa[2];
#pragma unroll
  for (int dt = 0; dt < 4; ++dt) voff[dt] = LDS_V + h * 16384 + (8 * hh + q4) * 256 + ((dt ^ q4) << 6) + blk * 32 + pp * 8;
#pragma unroll
  for (int s = 0; s < 8; ++s) koff[s] = LDS_K + h * 16384 + pir * 256 + (((2 * s + hh) ^ (pir & 15)) << 4);
#pragma unroll
  for (int s = 0; s < 2; ++s) wa[s] = (s ^ h) * 16384 + (8 * hh + q4) * 128 + ((h ^ (q4 >> 1)) << 6) + blk * 32 + pp * 8;
  int ooff = LDS_X + (qh * 16 + h * 8) * 1024 + lane * 16;
  int pofs = ((b * 8 * 16 + 4 * wid + (r >> 3)) * 16 + hp) * 512 + hh * 128 + (r & 7) * 16;
  asm volatile("" : "+v"(voff[0]), "+v"(voff[1]), "+v"(voff[2]), "+v"(voff[3]), "+v"(ooff), "+v"(wa[0]), "+v"(wa[1]), "+v"(pofs));
  asm volatile("" : "+v"(koff[0]), "+v"(koff[1]), "+v"(koff[2]), "+v"(koff[3]), "+v"(koff[4]), "+v"(koff[5]), "+v"(koff[6]), "+v"(koff[7]));
  const f32x16 zero16 = {0.f, 0.f, 0.f, 0.f, 0.f, 0.f, 0.f, 0.f, 0.f, 0.f, 0.f, 0.f, 0.f, 0.f, 0.f, 0.f};
  f32x16 accq[4] = {zero16, zero16, zero16, zero16};

  auto read_stage = [&](FragSet& f, int slot, int xu, int par) {
    LDSP char* wb = smem + slot * 16384;
    LDSP char* xb = smem + xu * 8192;
#pragma unroll
    for (int ks = 0; ks < 2; ++ks) {
      f.b[ks] = *(LDSP bf16x8*)(xb + xoff[2 * par + ks]);
#pragma unroll
      for (int dt = 0; dt < 4; ++dt) f.a[ks][dt] = tr_frag(wb + woff[dt], ks * 8192, ks * 8192 + 2048);
    }
  };
  auto step = [&](const FragSet& c, FragSet& n, int slot, int xu, int par) {
    LDSP char* wb = smem + slot * 16384;
    LDSP char* xb = smem + xu * 8192;
#pragma unroll
    for (int i = 0; i < 8; ++i) {
      accq[i & 3] = MFMA(c.a[i >> 2][i & 3], c.b[i >> 2], accq[i & 3]);
      SB0();
      if (i == 0) { n.b[0] = *(LDSP bf16x8*)(xb + xoff[2 * par]); n.a[0][0] = tr_frag(wb + woff[0], 0, 2048); }
      if (i == 1) { n.a[0][1] = tr_frag(wb + woff[1], 0, 2048); n.a[0][2] = tr_frag(wb + woff[2], 0, 2048); }
      if (i == 2) { n.a[0][3] = tr_frag(wb + woff[3], 0, 2048); n.b[1] = *(LDSP bf16x8*)(xb + xoff[2 * par + 1]); }
      if (i == 3) { n.a[1][0] = tr_frag(wb + woff[0], 8192, 10240); n.a[1][1] = tr_frag(wb + woff[1], 8192, 10240); }
      if (i == 4) { n.a[1][2] = tr_frag(wb + woff[2], 8192, 10240); n.a[1][3] = tr_frag(wb + woff[3], 8192, 10240); }
      SB0();
    }
  };

  FragSet f0, f1;
  CBAR();
  STAMP(1);
  read_stage(f0, 0, 0, 0);
  const unsigned nonce = __builtin_amdgcn_readfirstlane(*(LDSP unsigned*)(smem + LDS_X + 24576));
  WAITL0();
  {
    int sl = 0;
#pragma unroll 1
    for (int j = 0; j < 7; ++j) {
      step(f0, f1, sl + 1, j & 3, 1);
      WAITL0();
      CBAR();
      sl = (sl == 4) ? 0 : sl + 2;
      step(f1, f0, sl, (j + 1) & 3, 0);
      WAITL0();
    }
  }
  step(f0, f1, 3, 3, 1);
  WAITL0();
  CBAR();
  STAMP(12);
  bf16x8 kf[2][8], qf[8], vf[4][4], pf[4];
  f32x16 accs[2], acco[4];
#define QM(dt, ks) accq[dt] = MFMA(f1.a[ks][dt], f1.b[ks], accq[dt])
#define QP(i) qf[i] = pack8(accq[(i) >> 1], (i) & 1)
#define KR(kt, s) kf[kt][s] = *(LDSP bf16x8*)(smem + koff[s] + (kt) * 8192)
#define SM(kt, s) accs[kt] = MFMA(kf[kt][s], qf[s], (s) == 0 ? zero16 : accs[kt])
#define VR(dt, s) vf[dt][s] = tr_frag(smem + voff[dt], (s) * 4096, (s) * 4096 + 1024)
  const unsigned aofs = lane < 4 ? WS_A + b * 256 + (4 * wid + lane) * 4 : OOB;
  unsigned an = __builtin_amdgcn_raw_buffer_load_b32(prs, aofs, 0, 16);
  QM(0, 0); QM(0, 1); QM(1, 0); QM(1, 1); SB0();
  QP(0); SB0(); QM(2, 0); SB0();
  QP(1); SB0(); QM(2, 1); SB0();
  CBAR();
  KR(0, 0); KR(0, 1); KR(0, 2); KR(0, 3); SB0();
  QP(2); SB0(); QM(3, 0); SB0();
  KR(0, 4); KR(0, 5); KR(0, 6); KR(0, 7); SB0();
  QP(3); SB0(); QM(3, 1); SB0();
  QP(4); SB0(); SM(0, 0); SB0();
  QP(5); KR(1, 0); KR(1, 1); SB0(); SM(0, 1); SB0();
  QP(6); KR(1, 2); KR(1, 3); SB0(); SM(0, 2); SB0();
  QP(7); KR(1, 4); KR(1, 5); SB0(); SM(0, 3); SB0();
  KR(1, 6); KR(1, 7); SB0(); SM(0, 4); SM(0, 5); SM(0, 6); SM(0, 7); SM(1, 0); SM(1, 1); SM(1, 2); SM(1, 3); SB0();
  float mx = fmaxf(fmaxf(accs[0][0], accs[0][1]), accs[0][2]);
  mx = fmaxf(fmaxf(mx, accs[0][3]), accs[0][4]); mx = fmaxf(fmaxf(mx, accs[0][5]), accs[0][6]);
  SB0(); SM(1, 4); SB0();
  mx = fmaxf(fmaxf(mx, accs[0][7]), accs[0][8]); mx = fmaxf(fmaxf(mx, accs[0][9]), accs[0][10]); mx = fmaxf(fmaxf(mx, accs[0][11]), accs[0][12]);
  SB0(); SM(1, 5); SB0();
  mx = fmaxf(fmaxf(mx, accs[0][13]), accs[0][14]); mx = fmaxf(mx, accs[0][15]);
  SB0(); SM(1, 6); SM(1, 7); SB0();
  STAMP(2);
  {
    float m2 = fmaxf(fmaxf(accs[1][0], accs[1][1]), accs[1][2]);
#pragma unroll
    for (int i = 3; i < 9; i += 2) m2 = fmaxf(fmaxf(m2, accs[1][i]), accs[1][i + 1]);
#pragma unroll
    for (int i = 9; i < 15; i += 2) mx = fmaxf(fmaxf(mx, accs[1][i]), accs[1][i + 1]);
    mx = fmaxf(fmaxf(mx, m2), accs[1][15]);
    auto sw = __builtin_amdgcn_permlane32_swap(__float_as_uint(mx), __float_as_uint(mx), false, false);
    mx = fmaxf(__uint_as_float(sw[0]), __uint_as_float(sw[1]));
  }
  const float sc = 0.08838834764831845f * 1.4426950408889634f;
  const float mb = mx * sc;
  f32x2 sum2 = {0.f, 0.f}, sum3 = {0.f, 0.f};
#define EX(kt, i) do { f32x2 t = {accs[kt][i], accs[kt][(i) + 1]}; t = t * sc - mb; \
    t[0] = __builtin_amdgcn_exp2f(t[0]); t[1] = __builtin_amdgcn_exp2f(t[1]); \
    accs[kt][i] = t[0]; accs[kt][(i) + 1] = t[1]; if ((i) & 2) sum3 += t; else sum2 += t; } while (0)
#pragma unroll
  for (int i = 0; i < 16; i += 2) EX(0, i);
  WAITL0();
  CBAR();
#pragma unroll
  for (int i = 0; i < 8; ++i) { VR((2 * i) & 3, i >> 1); VR((2 * i + 1) & 3, i >> 1); SB0(); EX(1, 2 * i); SB0(); }
  sum2 += sum3;
  float sum = sum2[0] + sum2[1];
  {
    auto sw = __builtin_amdgcn_permlane32_swap(__float_as_uint(sum), __float_as_uint(sum), false, false);
    sum = __uint_as_float(sw[0]) + __uint_as_float(sw[1]);
  }
  const float inv = __builtin_amdgcn_rcpf(sum);
  SB0();
  bf16x8 of[16], wf[2][8];
  f32x16 acc[2];
#define PH(s, hf) do { _Pragma("unroll") for (int j = 4 * (hf); j < 4 * (hf) + 4; j += 2) { \
    f32x2 t = {accs[(s) >> 1][8 * ((s) & 1) + j], accs[(s) >> 1][8 * ((s) & 1) + j + 1]}; t *= inv; \
    pf[s][j] = (__bf16)t[0]; pf[s][j + 1] = (__bf16)t[1]; } } while (0)
#define OM(dt, s) acco[dt] = MFMA(vf[dt][s], pf[s], (s) == 0 ? zero16 : acco[dt])
#define OW(i) do { of[i] = pack8(acco[(i) >> 1], (i) & 1); *(LDSP bf16x8*)(smem + ooff + (i) * 1024) = of[i]; } while (0)
  PH(0, 0); PH(0, 1);
  asm volatile("" : "+v"(an));
  WAITL0();
  CBAR();
  OM(0, 0); SB0(); PH(1, 0); SB0();
  OM(1, 0); SB0(); PH(1, 1); SB0();
  OM(2, 0); SB0(); PH(2, 0); SB0();
  OM(3, 0); SB0(); PH(2, 1); SB0();
  OM(0, 1); SB0(); PH(3, 0); SB0();
  OM(1, 1); SB0(); PH(3, 1); SB0();
  OM(2, 1); SB0(); OM(3, 1); SB0(); OM(0, 2); SB0(); OM(0, 3); SB0(); OM(1, 2); SB0(); OM(1, 3); SB0();
  OW(0); SB0(); OM(2, 2); SB0();
  OW(1); SB0(); OM(2, 3); SB0();
  OW(2); SB0(); OM(3, 2); SB0();
  OW(3); SB0(); OM(3, 3); SB0();
  OW(4); OW(5); SB0(); OW(6); OW(7);
  WAITL0();
  STAMP(3);
  CBAR();
  STAMP(4);
#define WR(u, i) wf[(u) & 1][i] = tr_frag(wp, (i) * 2048, (i) * 2048 + 512)
#define PM(u, i) acc[((u) >> 1) & 1] = MFMA(wf[(u) & 1][i], of[8 * ((u) & 1) + (i)], (((u) & 1) == 0 && (i) == 0) ? zero16 : acc[((u) >> 1) & 1])
#define PST(c, a) __builtin_amdgcn_raw_buffer_store_b128(__builtin_bit_cast(u32x4, pack8(acc[(c) & 1], a)), prs, pofs + (c) * 131072 + (a) * 256, 0, PAUX)
  const unsigned fofs = lane < 4 ? WS_F + ((b * 16 + hp) * 4 + wid) * 256 + lane * 4 : OOB;
#define FLAG(n) __builtin_amdgcn_raw_buffer_store_b32((an << 4) | (n), prs, fofs, 0, 16)
  LDSP char* wp = smem + wa[0] + PBLK(0) * 32768;
#pragma unroll
  for (int i = 0; i < 8; ++i) WR(0, i);
  SB0();
#pragma unroll
  for (int u = 0; u < 16; ++u) {
    if ((u & 1) && u < (EARLYREL ? 11 : 13)) { WAITL0(); CBAR(); }
    if (u < 15) { int wo_ = wa[(u + 1) & 1] + PBLK((u + 1) >> 1) * 32768; asm volatile("" : "+v"(wo_)); wp = smem + wo_; }
    const int c = (u >> 1) - 1;
#pragma unroll
    for (int i = 0; i < 8; ++i) {
      PM(u, i); SB0();
      if (u == 0) { if (i < 4) { of[8 + 2 * i] = *(LDSP bf16x8*)(smem + (ooff ^ 8192) + 2 * i * 1024); of[9 + 2 * i] = *(LDSP bf16x8*)(smem + (ooff ^ 8192) + (2 * i + 1) * 1024); } }
      if (u < 15) { if (i < 4) { WR(u + 1, 2 * i); WR(u + 1, 2 * i + 1); } }
      if ((u & 1) == 0 && u > 0) {
        if (i == 4) PST(c, 0);
        if (i == 5) PST(c, 1);
        if (i == 6 && c == 5) { WAITV(4); FLAG(4); }
      }
      SB0();
    }
    if ((u & 3) == 3) STAMP(5 + (u >> 2));
  }
  PST(7, 0); PST(7, 1);
  WAITV(5); FLAG(6);
  WAITV(1); FLAG(8);
  STAMP(9);
  {
    const unsigned vofs = lane < 4 ? WS_F + ((b * 16 + 4 * wid + lane) * 4 + (hp >> 2)) * 256 + (hp & 3) * 4 : OOB;
#pragma unroll 1
    for (int spin = 0; spin < SPIN_CAP; ++spin) {
      const unsigned f = ld_sc1(prs, vofs);
      const bool ok = __all(lane >= 4 || (f >> 4) == nonce);
      const unsigned a2 = ld_sc1(prs, aofs);
      if (__any(a2 != an)) { an = a2; FLAG(8); }
      if (ok) break;
      __builtin_amdgcn_s_sleep(8);
    }
  }
}

extern "C" void kernel_launch(void* const* d_in, const int* in_sizes, int n_in, void* d_out, int out_size, void* d_ws,
                              size_t ws_size, hipStream_t stream) {
  fused_kernel<<<256, 768, 0, stream>>>((const char*)d_in[0], (const char*)d_in[1], (const char*)d_in[2],
                                        (const char*)d_in[3], (const char*)d_in[4], (char*)d_ws + (ws_size - (20u  __H0__ )), (char*)d_out);
}
```

```cpp
#include <hip/hip_runtime.h>
#include <hip/hip_bf16.h>

typedef __bf16 bf16x4 __attribute__((ext_vector_type(4)));
typedef __bf16 bf16x8 __attribute__((ext_vector_type(8)));
typedef float f32x16 __attribute__((ext_vector_type(16)));
typedef float f32x2 __attribute__((ext_vector_type(2)));
typedef unsigned u32x4 __attribute__((ext_vector_type(4)));
typedef unsigned u32x2 __attribute__((ext_vector_type(2)));
typedef float f32x4 __attribute__((ext_vector_type(4)));
#define LDSP __attribute__((address_space(3)))
#define DI __device__ __forceinline__
#define MFMA(a, b, c) __builtin_amdgcn_mfma_f32_32x32x16_bf16((a), (b), (c), 0, 0, 0)
#define BAR() do { asm volatile("" ::: "memory"); __builtin_amdgcn_s_barrier(); asm volatile("" ::: "memory"); } while (0)
#define WAITV(n) asm volatile("s_waitcnt vmcnt(%0)" :: "i"(n) : "memory")
#define SB0() __builtin_amdgcn_sched_barrier(0)
#define WAITL0() do { asm volatile("" ::: "memory"); SB0(); __builtin_amdgcn_s_waitcnt(0xC07F); SB0(); asm volatile("" ::: "memory"); } while (0)
#define PAUX 16
#define WOEARLY 1
#define RAUX 16
#define FL7 0
#define PRE 2
#define RD1 0
#define KVAUX 2

constexpr int LDS_X = 0;
constexpr int LDS_K = 32768;
constexpr int LDS_W = 65536;
constexpr int LDS_V = 131072;
constexpr int LDS_TOTAL = 163840;
constexpr int WS_F = 16 << 20;
constexpr int WS_A = WS_F + (256 << 10);
constexpr int WS_BYTES = WS_A + 4096;
constexpr unsigned OOB = 0x7ffffff0u;
constexpr int SPIN_CAP = 1 << 16;
#define PDELAY 16
#define EARLYREL 0
#define SPF 0
#define OAUX 2
#define LFIRST 0
#define FMODE 2
#define ORD 0
#define STAG 0
#define EARLY 0
#define P7 1
#define PSLEEP 4
#define PBLK(pb) ((0x41324132u >> (4 * (pb))) & 15)

DI bf16x8 tr_frag(LDSP char* p, int off_lo, int off_hi) {
  bf16x4 a = __builtin_amdgcn_ds_read_tr16_b64_v4bf16((LDSP bf16x4*)(p + off_lo));
  bf16x4 b = __builtin_amdgcn_ds_read_tr16_b64_v4bf16((LDSP bf16x4*)(p + off_hi));
  return __builtin_shufflevector(a, b, 0, 1, 2, 3, 4, 5, 6, 7);
}
DI unsigned ld_sc1(__amdgpu_buffer_rsrc_t rs, unsigned ofs) {
  unsigned v;
  asm volatile("buffer_load_dword %0, %1, %2, 0 offen sc1\n\ts_waitcnt vmcnt(0)" : "=&v"(v) : "v"(ofs), "s"(rs) : "memory");
  return v;
}
DI bf16x8 pack8(const f32x16& x, int s) {
  bf16x8 p;
#pragma unroll
  for (int j = 0; j < 8; ++j) p[j] = (__bf16)x[8 * s + j];
  return p;
}

#define TS()
#define TSREC()
#define TSDUMP()
#define CBAR() BAR()
#define LT(k)
#define STAMP(k)
#define RSTAMP(k)
struct FragSet { bf16x8 a[2][4]; bf16x8 b[2]; };

__global__ __launch_bounds__(768, 3) void fused_kernel(const char* __restrict__ x, const char* __restrict__ kc,
                                                       const char* __restrict__ vc, const char* __restrict__ wq,
                                                       const char* __restrict__ wo, char* part, char* out) {
  __shared__ __attribute__((aligned(16))) char smem_[LDS_TOTAL];
  LDSP char* smem = (LDSP char*)smem_;
  const int wid0 = __builtin_amdgcn_readfirstlane(threadIdx.x >> 6);
  const int wid = wid0 < 4 ? wid0 : wid0 - 12;
  const int lane = threadIdx.x & 63;
  const int bid = blockIdx.x;
  const int hp = (bid & 7) * 2 + ((bid >> 3) & 1);
  const int b = bid >> 4;
  const int grpB = b & 1;
  const int u0 = b >> 1;
  const int swp = b & 1;

  if (wid < 0) {
    const int lw = wid + 8;
    LT(0);
    const int hi = lane >> 5;
    const auto rq = __builtin_amdgcn_make_buffer_rsrc((void*)wq, 0, 4 << 20, 0x00020000);
    const auto ro = __builtin_amdgcn_make_buffer_rsrc((void*)wo, 0, 4 << 20, 0x00020000);
    const auto rx = __builtin_amdgcn_make_buffer_rsrc((void*)x, 0, 1 << 20, 0x00020000);
    const auto rk = __builtin_amdgcn_make_buffer_rsrc((void*)kc, 0, 8 << 20, 0x00020000);
    const auto rv = __builtin_amdgcn_make_buffer_rsrc((void*)vc, 0, 8 << 20, 0x00020000);
    const int wq_e = (4 * lw + hi) * 8192 + hp * 512 + (((lane & 31) ^ (hi << 2)) << 4);
    const int wq_o = (4 * lw + 2 + hi) * 8192 + hp * 512 + (((lane & 31) ^ ((2 + hi) << 2)) << 4);
    const int xg = b * 65536 + (8 * lw + (lane >> 3)) * 1024 + (((lane & 7) ^ (4 * (lw & 1) + (lane >> 4))) << 4);
#define DMA(rs, vo, so, l, aux) __builtin_amdgcn_raw_ptr_buffer_load_lds(rs, (LDSP unsigned*)(l), 16, vo, so, 0, aux)
#define DMA4(rs, vo, so, l) __builtin_amdgcn_raw_ptr_buffer_load_lds(rs, (LDSP unsigned*)(l), 4, vo, so, 0, 0)
    auto issue_pair = [&](int j, int blkofs) {
      const int ku = (u0 + j) & 7;
      LDSP char* d = smem + blkofs + lw * 2048;
#pragma unroll
      for (int sg = 0; sg < 2; ++sg) {
        const int go = (2 * ku + (sg ^ swp)) * 32 * 8192;
        DMA(rq, wq_e, go, d + sg * 16384, 0);
        DMA(rq, wq_o, go, d + sg * 16384 + 1024, 0);
        if (sg == 0) DMA(rx, xg, ku * 128, smem + LDS_X + (j & 3) * 8192 + lw * 1024, 0);
      }
    };
    issue_pair(0, LDS_W);
    const int c16 = ((lane & 15) ^ ((lane >> 4) << 2)) << 4;
    const int wog = (hp * 256 + 32 * lw + (lane >> 3)) * 1024 + (((lane & 7) ^ (((lane >> 4) & 1) << 2)) << 4);
    const int kvb = (b * 32 + hp * 2) * 16384;
    const int vg = kvb + (16 * lw + (lane >> 4)) * 256 + c16;
    const int kg = kvb + (4 * lw + (lane >> 4)) * 256 + (((lane & 15) ^ (4 * (lw & 3) + (lane >> 4))) << 4);
    auto issue_k = [&]() {
#pragma unroll
      for (int i = 0; i < 4; ++i) DMA(rk, kg, i * 8192, smem + LDS_K + i * 8192 + lw * 1024, KVAUX);
    };
    auto issue_v = [&]() {
#pragma unroll
      for (int i = 0; i < 4; ++i) DMA(rv, vg, i * 1024, smem + LDS_V + lw * 4096 + i * 1024, KVAUX);
    };
    auto issue_wo = [&](int c) {
      LDSP char* d = smem + PBLK(c) * 32768 + lw * 4096;
#pragma unroll
      for (int i = 0; i < 4; ++i) DMA(ro, wog, c * 128 + i * 8192, d + i * 1024, 0);
    };
    int pfo = grpB ? (hp * 256 + (b >> 1) * 32 + lw * 4) * 1024 + lane * 64
                   : b * 65536 + ((hp & 1) * 32 + lw * 4) * 1024 + lane * 64;
    asm volatile("" : "+v"(pfo));
    if (!grpB) {
      DMA4(rx, pfo, 65536, smem + LDS_K + lw * 1024);
      DMA4(rx, pfo, 0, smem + LDS_K + lw * 1024);
      issue_k();
    }
    issue_pair(1, LDS_W + 32768);
    WAITV(5); BAR();
    unsigned nonce;
    asm volatile("ds_read_b32 %0, %1\n\ts_waitcnt lgkmcnt(0)" : "=v"(nonce) : "v"(LDS_X + 24576) : "memory");
    nonce = __builtin_amdgcn_readfirstlane(nonce);
    {
      int bo = LDS_W + 65536;
#pragma unroll 1
      for (int j = 2; j < 8; ++j) {
        issue_pair(j, bo);
        bo = (bo == LDS_W + 65536) ? LDS_W : bo + 32768;
        WAITV(5); BAR();
      }
    }
    if (grpB) {
      DMA4(ro, pfo, 0, smem + LDS_K + lw * 1024);
      issue_k();
      WAITV(5); BAR();
      issue_v();
      BAR();
      WAITV(4); BAR();
    } else {
      issue_v();
      WAITV(4); BAR();
      BAR();
      BAR();
    }
    issue_wo(0);
    WAITV(4); BAR();
    issue_wo(1); issue_wo(2);
    WAITV(8); BAR();
    issue_wo(3);
    WAITV(8); BAR();
    if (lw == 7) *(volatile LDSP int*)(smem + LDS_X + 8192) = 0;
#pragma unroll 1
    for (int c = 4; c < 8; ++c) {
      issue_wo(c);
      WAITV(8); BAR();
    }
    WAITV(4); BAR();
    WAITV(0); WAITL0(); BAR();
    {
      const auto prs = __builtin_amdgcn_make_buffer_rsrc(part, 0, WS_BYTES, 0x00020000);
      const auto rout = __builtin_amdgcn_make_buffer_rsrc(out, 0, 1 << 20, 0x00020000);
      const int fofs = WS_F + ((b * 16 + 2 * lw + (lane & 1)) * 4 + (hp >> 2)) * 256 + (hp & 3) * 4;
      const int inb = ((b * 16 + hp) * 16 + 2 * lw) * 4096;
      u32x4 v[2][4];
      unsigned got = 0;
      __builtin_amdgcn_s_sleep(PDELAY);
#pragma unroll 1
      for (int spin = 0; spin < SPIN_CAP; ++spin) {
        const unsigned f = ld_sc1(prs, (lane < 2 && !((got >> lane) & 1)) ? fofs : OOB);
        const unsigned rdy = (unsigned)__ballot(lane < 2 && f == ((nonce << 4) | 8)) | (spin == SPIN_CAP - 1 ? 3u : 0u);
        const unsigned nw = rdy & ~got;
        if (nw & 1) {
#pragma unroll
          for (int i = 0; i < 4; ++i) v[0][i] = __builtin_amdgcn_raw_buffer_load_b128(prs, lane * 16 + i * 1024, inb, 16);
        }
        if (nw & 2) {
#pragma unroll
          for (int i = 0; i < 4; ++i) v[1][i] = __builtin_amdgcn_raw_buffer_load_b128(prs, lane * 16 + i * 1024, inb + 4096, 16);
        }
        got |= rdy;
        if (got == 3) break;
        __builtin_amdgcn_s_sleep(PSLEEP);
      }
      if (lw == 0) RSTAMP(13);
      if (lw == 7) RSTAMP(15);
      LDSP f32x4* slab = (LDSP f32x4*)(smem + LDS_W + lw * 8192) + lane;
#pragma unroll
      for (int i = 0; i < 4; ++i)
#pragma unroll
        for (int e = 0; e < 2; ++e) {
          f32x4 t;
          t[0] = __uint_as_float(v[0][i][2 * e] << 16) + __uint_as_float(v[1][i][2 * e] << 16);
          t[1] = __uint_as_float(v[0][i][2 * e] & 0xffff0000u) + __uint_as_float(v[1][i][2 * e] & 0xffff0000u);
          t[2] = __uint_as_float(v[0][i][2 * e + 1] << 16) + __uint_as_float(v[1][i][2 * e + 1] << 16);
          t[3] = __uint_as_float(v[0][i][2 * e + 1] & 0xffff0000u) + __uint_as_float(v[1][i][2 * e + 1] & 0xffff0000u);
          slab[(2 * i + e) * 64] = t;
        }
      WAITL0();
      if (lane == 0) __hip_atomic_fetch_add((LDSP int*)(smem + LDS_X + 8192), 1, __ATOMIC_RELAXED, __HIP_MEMORY_SCOPE_WORKGROUP);
#pragma unroll 1
      for (int spin = 0; spin < (SPIN_CAP << 6); ++spin) {
        if (__builtin_amdgcn_readfirstlane(*(volatile LDSP int*)(smem + LDS_X + 8192)) >= 8) break;
        __builtin_amdgcn_s_sleep(1);
      }
      asm volatile("" ::: "memory");
      if (lw == 0) RSTAMP(14);
      LDSP f32x4* src = (LDSP f32x4*)(smem + LDS_W) + lw * 64 + lane;
      f32x4 sacc = src[0];
#pragma unroll
      for (int w = 1; w < 8; ++w) sacc += src[w * 512];
      bf16x4 o;
#pragma unroll
      for (int e = 0; e < 4; ++e) o[e] = (__bf16)sacc[e];
      const int c = 2 * (lw >> 1) + (lane >> 5);
      const int row = b * 64 + 32 * ((hp >> 2) & 1) + 8 * (hp & 3) + (lane & 7);
      const int col = 64 * c + 32 * (hp >> 3) + 16 * ((lane >> 4) & 1) + 8 * ((lane >> 3) & 1) + 4 * (lw & 1);
      __builtin_amdgcn_raw_buffer_store_b64(__builtin_bit_cast(u32x2, o), rout, row * 1024 + col * 2, 0, OAUX);
    }
    return;
  }

  const int h = wid >> 1, qh = wid & 1;
  const int r = lane & 31, hh = lane >> 5;
  const int blk = (lane >> 4) & 1, q4 = (lane >> 2) & 3, p4 = lane & 3;
  const int pp = ((p4 & 1) << 1) | (p4 >> 1);
  const int pir = (r & 19) | ((r & 4) << 1) | ((r & 8) >> 1);
  STAMP(0);
  const auto prs = __builtin_amdgcn_make_buffer_rsrc(part, 0, WS_BYTES, 0x00020000);
  if (wid == 0) {
    const unsigned f = ld_sc1(prs, WS_F + ((b * 16 + (lane & 15)) * 4 + (hp >> 2)) * 256 + (hp & 3) * 4) >> 4;
    unsigned cand = 0;
#pragma unroll 1
    while (__any(f == cand)) ++cand;
    __builtin_amdgcn_raw_buffer_store_b32(cand, prs, lane == 0 ? WS_A + b * 256 + hp * 4 : OOB, 0, 16);
    *(LDSP unsigned*)(smem + LDS_X + 24576) = cand;
  }
  WAITL0();

  int woff[4];
#pragma unroll
  for (int dt = 0; dt < 4; ++dt) woff[dt] = LDS_W + (8 * hh + q4) * 512 + h * 256 + ((dt ^ q4) << 6) + blk * 32 + pp * 8;
  const int xrow = 32 * qh + r;
  int xoff[4];
#pragma unroll
  for (int ks = 0; ks < 4; ++ks) xoff[ks] = LDS_X + xrow * 128 + (((2 * (ks ^ (2 * swp)) + hh) ^ ((xrow >> 1) & 7)) << 4);
  int voff[4], koff[8], wa[2];
#pragma unroll
  for (int dt = 0; dt < 4; ++dt) voff[dt] = LDS_V + h * 16384 + (8 * hh + q4) * 256 + ((dt ^ q4) << 6) + blk * 32 + pp * 8;
#pragma unroll
  for (int s = 0; s < 8; ++s) koff[s] = LDS_K + h * 16384 + pir * 256 + (((2 * s + hh) ^ (pir & 15)) << 4);
#pragma unroll
  for (int s = 0; s < 2; ++s) wa[s] = (s ^ h) * 16384 + (8 * hh + q4) * 128 + ((h ^ (q4 >> 1)) << 6) + blk * 32 + pp * 8;
  int ooff = LDS_X + (qh * 16 + h * 8) * 1024 + lane * 16;
  int pofs = ((b * 16 + 4 * wid + (r >> 3)) * 16 + hp) * 4096 + hh * 128 + (r & 7) * 16;
  asm volatile("" : "+v"(voff[0]), "+v"(voff[1]), "+v"(voff[2]), "+v"(voff[3]), "+v"(ooff), "+v"(wa[0]), "+v"(wa[1]), "+v"(pofs));
  asm volatile("" : "+v"(koff[0]), "+v"(koff[1]), "+v"(koff[2]), "+v"(koff[3]), "+v"(koff[4]), "+v"(koff[5]), "+v"(koff[6]), "+v"(koff[7]));
  const f32x16 zero16 = {0.f, 0.f, 0.f, 0.f, 0.f, 0.f, 0.f, 0.f, 0.f, 0.f, 0.f, 0.f, 0.f, 0.f, 0.f, 0.f};
  f32x16 accq[4] = {zero16, zero16, zero16, zero16};

  auto read_stage = [&](FragSet& f, int slot, int xu, int par) {
    LDSP char* wb = smem + slot * 16384;
    LDSP char* xb = smem + xu * 8192;
#pragma unroll
    for (int ks = 0; ks < 2; ++ks) {
      f.b[ks] = *(LDSP bf16x8*)(xb + xoff[2 * par + ks]);
#pragma unroll
      for (int dt = 0; dt < 4; ++dt) f.a[ks][dt] = tr_frag(wb + woff[dt], ks * 8192, ks * 8192 + 2048);
    }
  };
#define MM(c, i) accq[(i) & 3] = MFMA(c.a[(i) >> 2][(i) & 3], c.b[(i) >> 2], accq[(i) & 3])
  auto rd = [&](FragSet& n, LDSP char* wb, LDSP char* xb, int par, int g) {
    if (g == 0) { n.b[0] = *(LDSP bf16x8*)(xb + xoff[2 * par]); n.a[0][0] = tr_frag(wb + woff[0], 0, 2048); }
    if (g == 1) { n.a[0][1] = tr_frag(wb + woff[1], 0, 2048); n.a[0][2] = tr_frag(wb + woff[2], 0, 2048); }
    if (g == 2) { n.a[0][3] = tr_frag(wb + woff[3], 0, 2048); n.b[1] = *(LDSP bf16x8*)(xb + xoff[2 * par + 1]); }
    if (g == 3) { n.a[1][0] = tr_frag(wb + woff[0], 8192, 10240); n.a[1][1] = tr_frag(wb + woff[1], 8192, 10240); }
    if (g == 4) { n.a[1][2] = tr_frag(wb + woff[2], 8192, 10240); n.a[1][3] = tr_frag(wb + woff[3], 8192, 10240); }
  };

  FragSet f0, f1;
  asm volatile("" : "+v"(accq[0]), "+v"(accq[1]), "+v"(accq[2]), "+v"(accq[3]));
  CBAR();
  STAMP(1);
  read_stage(f0, 0, 0, 0);
  const unsigned nonce = __builtin_amdgcn_readfirstlane(*(LDSP unsigned*)(smem + LDS_X + 24576));
  SB0();
#pragma unroll
  for (int i = 0; i < 8; ++i) { MM(f0, i); SB0(); rd(f1, smem + 16384, smem, 1, i); SB0(); }
#pragma unroll
  for (int i = 0; i < PRE; ++i) { MM(f1, i); SB0(); }
  WAITL0();
  {
    int sl = 0;
#pragma unroll 1
    for (int j = 1; j < 8; ++j) {
      CBAR();
      sl = (sl == 4) ? 0 : sl + 2;
      LDSP char* wb = smem + sl * 16384;
      LDSP char* xb = smem + (j & 3) * 8192;
      rd(f0, wb, xb, 0, 0); SB0();
#pragma unroll
      for (int i = PRE; i < 8; ++i) { MM(f1, i); SB0(); rd(f0, wb, xb, 0, i - PRE + 1); SB0(); }
#pragma unroll
      for (int i = 0; i < 8; ++i) { MM(f0, i); SB0(); rd(f1, wb + 16384, xb, 1, i - RD1); SB0(); }
#pragma unroll
      for (int i = 0; i < PRE; ++i) { MM(f1, i); SB0(); }
      WAITL0();
    }
  }
  CBAR();
  STAMP(12);
  bf16x8 kf[2][8], qf[8], vf[4][4], pf[4];
  f32x16 accs[2], acco[4];
#define QM(dt, ks) do { if ((ks) || (dt) >= PRE) accq[dt] = MFMA(f1.a[ks][dt], f1.b[ks], accq[dt]); } while (0)
#define QP(i) qf[i] = pack8(accq[(i) >> 1], (i) & 1)
#define KR(kt, s) kf[kt][s] = *(LDSP bf16x8*)(smem + koff[s] + (kt) * 8192)
#define SM(kt, s) accs[kt] = MFMA(kf[kt][s], qf[s], (s) == 0 ? zero16 : accs[kt])
#define VR(dt, s) vf[dt][s] = tr_frag(smem + voff[dt], (s) * 4096, (s) * 4096 + 1024)
  const unsigned aofs = lane < 4 ? WS_A + b * 256 + (4 * wid + lane) * 4 : OOB;
  unsigned an = __builtin_amdgcn_raw_buffer_load_b32(prs, aofs, 0, 16);
  QM(0, 0); QM(0, 1); QM(1, 0); QM(1, 1); SB0();
  QP(0); SB0(); QM(2, 0); SB0();
  QP(1); SB0(); QM(2, 1); SB0();
  CBAR();
  KR(0, 0); KR(0, 1); KR(0, 2); KR(0, 3); SB0();
  QP(2); SB0(); QM(3, 0); SB0();
  KR(0, 4); KR(0, 5); KR(0, 6); KR(0, 7); SB0();
  QP(3); SB0(); QM(3, 1); SB0();
  QP(4); SB0(); SM(0, 0); SB0();
  QP(5); KR(1, 0); KR(1, 1); SB0(); SM(0, 1); SB0();
  QP(6); KR(1, 2); KR(1, 3); SB0(); SM(0, 2); SB0();
  QP(7); KR(1, 4); KR(1, 5); SB0(); SM(0, 3); SB0();
  KR(1, 6); KR(1, 7); SB0(); SM(0, 4); SM(0, 5); SM(0, 6); SM(0, 7); SB0(); SM(1, 0); SM(1, 1); SM(1, 2); SM(1, 3); SB0();
  float mx = fmaxf(fmaxf(accs[0][0], accs[0][1]), accs[0][2]);
  mx = fmaxf(fmaxf(mx, accs[0][3]), accs[0][4]); mx = fmaxf(fmaxf(mx, accs[0][5]), accs[0][6]);
  asm volatile("" : "+v"(mx)); SB0(); SM(1, 4); SB0();
  mx = fmaxf(fmaxf(mx, accs[0][7]), accs[0][8]); mx = fmaxf(fmaxf(mx, accs[0][9]), accs[0][10]); mx = fmaxf(fmaxf(mx, accs[0][11]), accs[0][12]);
  asm volatile("" : "+v"(mx)); SB0(); SM(1, 5); SB0();
  mx = fmaxf(fmaxf(mx, accs[0][13]), accs[0][14]); mx = fmaxf(mx, accs[0][15]);
  asm volatile("" : "+v"(mx)); SB0(); SM(1, 6); SM(1, 7); SB0();
  STAMP(2);
  {
    float m2 = fmaxf(fmaxf(accs[1][0], accs[1][1]), accs[1][2]);
#pragma unroll
    for (int i = 3; i < 9; i += 2) m2 = fmaxf(fmaxf(m2, accs[1][i]), accs[1][i + 1]);
#pragma unroll
    for (int i = 9; i < 15; i += 2) mx = fmaxf(fmaxf(mx, accs[1][i]), accs[1][i + 1]);
    mx = fmaxf(fmaxf(mx, m2), accs[1][15]);
    auto sw = __builtin_amdgcn_permlane32_swap(__float_as_uint(mx), __float_as_uint(mx), false, false);
    mx = fmaxf(__uint_as_float(sw[0]), __uint_as_float(sw[1]));
  }
  const float sc = 0.08838834764831845f * 1.4426950408889634f;
  const float mb = mx * sc;
  f32x2 sum2 = {0.f, 0.f}, sum3 = {0.f, 0.f};
#define EX(kt, i) do { f32x2 t = {accs[kt][i], accs[kt][(i) + 1]}; t = t * sc - mb; \
    t[0] = __builtin_amdgcn_exp2f(t[0]); t[1] = __builtin_amdgcn_exp2f(t[1]); \
    accs[kt][i] = t[0]; accs[kt][(i) + 1] = t[1]; if ((i) & 2) sum3 += t; else sum2 += t; } while (0)
#pragma unroll
  for (int i = 0; i < 16; i += 2) EX(0, i);
  WAITL0();
  CBAR();
#pragma unroll
  for (int i = 0; i < 4; ++i) { VR(0, i); VR(1, i); VR(2, i); VR(3, i); SB0(); EX(1, 4 * i); EX(1, 4 * i + 2); SB0(); }
  sum2 += sum3;
  float sum = sum2[0] + sum2[1];
  {
    auto sw = __builtin_amdgcn_permlane32_swap(__float_as_uint(sum), __float_as_uint(sum), false, false);
    sum = __uint_as_float(sw[0]) + __uint_as_float(sw[1]);
  }
  const float inv = __builtin_amdgcn_rcpf(sum);
  SB0();
  bf16x8 of[16], wf[2][8];
  f32x16 acc[2];
#define PH(s, hf) do { _Pragma("unroll") for (int j = 4 * (hf); j < 4 * (hf) + 4; j += 2) { \
    f32x2 t = {accs[(s) >> 1][8 * ((s) & 1) + j], accs[(s) >> 1][8 * ((s) & 1) + j + 1]}; t *= inv; \
    pf[s][j] = (__bf16)t[0]; pf[s][j + 1] = (__bf16)t[1]; } } while (0)
#define OM(dt, s) acco[dt] = MFMA(vf[dt][s], pf[s], (s) == 0 ? zero16 : acco[dt])
#define OW(i) do { of[i] = pack8(acco[(i) >> 1], (i) & 1); *(LDSP bf16x8*)(smem + ooff + (i) * 1024) = of[i]; } while (0)
  PH(0, 0); PH(0, 1);
  asm volatile("" : "+v"(an));
  SB0();
  OM(0, 0); SB0(); PH(1, 0); SB0();
  OM(1, 0); SB0(); PH(1, 1); SB0();
  OM(2, 0); SB0(); PH(2, 0); SB0();
  OM(3, 0); SB0(); PH(2, 1); SB0();
  OM(0, 1); SB0(); PH(3, 0); SB0();
  OM(1, 1); SB0(); PH(3, 1); SB0();
  OM(2, 1); SB0(); OM(3, 1); SB0(); OM(0, 2); SB0(); OM(0, 3); SB0(); OM(1, 2); SB0(); OM(1, 3); SB0();
  OW(0); SB0(); OM(2, 2); SB0();
  OW(1); SB0(); OM(2, 3); SB0();
  OW(2); SB0(); OM(3, 2); SB0();
  OW(3); SB0(); OM(3, 3); SB0();
  OW(4); OW(5); SB0(); OW(6); OW(7);
  WAITL0();
  STAMP(3);
  CBAR();
  STAMP(4);
#define WR(u, i) wf[(u) & 1][i] = tr_frag(wp, (i) * 2048, (i) * 2048 + 512)
#define PM(u, i) acc[((u) >> 1) & 1] = MFMA(wf[(u) & 1][i], of[8 * ((u) & 1) + (i)], (((u) & 1) == 0 && (i) == 0) ? zero16 : acc[((u) >> 1) & 1])
#define PST(c, a) __builtin_amdgcn_raw_buffer_store_b128(__builtin_bit_cast(u32x4, pack8(acc[(c) & 1], a)), prs, pofs + (c) * 512 + (a) * 256, 0, PAUX)
  const unsigned fofs = lane < 4 ? WS_F + ((b * 16 + hp) * 4 + wid) * 256 + lane * 4 : OOB;
#define FLAG(n) __builtin_amdgcn_raw_buffer_store_b32((an << 4) | (n), prs, fofs, 0, 16)
  LDSP char* wp = smem + wa[0] + PBLK(0) * 32768;
  bf16x8 pk[2];
#pragma unroll
  for (int i = 0; i < 8; ++i) WR(0, i);
  SB0();
#pragma unroll
  for (int u = 0; u < 16; ++u) {
    if ((u & 1) && u < (P7 ? 15 : 13)) { WAITL0(); CBAR(); }
    if (u < 15) { int wo_ = wa[(u + 1) & 1] + PBLK((u + 1) >> 1) * 32768; asm volatile("" : "+v"(wo_)); wp = smem + wo_; }
    const int c = (u >> 1) - 1;
#pragma unroll
    for (int i = 0; i < 8; ++i) {
      PM(u, i); SB0();
      if (u == 0) { if (i < 4) { of[8 + 2 * i] = *(LDSP bf16x8*)(smem + (ooff ^ 8192) + 2 * i * 1024); of[9 + 2 * i] = *(LDSP bf16x8*)(smem + (ooff ^ 8192) + (2 * i + 1) * 1024); } }
      if (u < 15) { if (i < 2) { WR(u + 1, 2 * i); WR(u + 1, 2 * i + 1); } else if (i < 6) WR(u + 1, i + 2); }
      if ((u & 1) == 0 && u > 0) {
        if (i == 4) PST(c, 0);
        if (i == 5) PST(c, 1);
      }
      SB0();
    }
    if ((u & 3) == 3) STAMP(5 + (u >> 2));
  }
  PST(7, 0); PST(7, 1);
  WAITV(0);
#if !(PAUX & 16)
  __builtin_amdgcn_fence(__ATOMIC_RELEASE, "agent");
  WAITV(0);
#endif
  FLAG(8);
  STAMP(9);
  {
    const unsigned vofs = lane < 4 ? WS_F + ((b * 16 + 4 * wid + lane) * 4 + (hp >> 2)) * 256 + (hp & 3) * 4 : OOB;
#pragma unroll 1
    for (int spin = 0; spin < SPIN_CAP; ++spin) {
      const unsigned f = ld_sc1(prs, vofs);
      const bool ok = __all(lane >= 4 || (f >> 4) == nonce);
      const unsigned a2 = ld_sc1(prs, aofs);
      if (__any(a2 != an)) { an = a2; FLAG(8); }
      if (ok) break;
      __builtin_amdgcn_s_sleep(8);
    }
  }
}

extern "C" void kernel_launch(void* const* d_in, const int* in_sizes, int n_in, void* d_out, int out_size, void* d_ws,
                              size_t ws_size, hipStream_t stream) {
  fused_kernel<<<256, 768, 0, stream>>>((const char*)d_in[0], (const char*)d_in[1], (const char*)d_in[2],
                                        (const char*)d_in[3], (const char*)d_in[4], (char*)d_ws, (char*)d_out);
}
```

```cpp
#include <hip/hip_runtime.h>
#include <hip/hip_bf16.h>

typedef __bf16 bf16x4 __attribute__((ext_vector_type(4)));
typedef __bf16 bf16x8 __attribute__((ext_vector_type(8)));
typedef float f32x16 __attribute__((ext_vector_type(16)));
typedef float f32x2 __attribute__((ext_vector_type(2)));
typedef unsigned u32x4 __attribute__((ext_vector_type(4)));
typedef unsigned u32x2 __attribute__((ext_vector_type(2)));
typedef float f32x4 __attribute__((ext_vector_type(4)));
#define LDSP __attribute__((address_space(3)))
#define DI __device__ __forceinline__
#define MFMA(a, b, c) __builtin_amdgcn_mfma_f32_32x32x16_bf16((a), (b), (c), 0, 0, 0)
#define BAR() do { asm volatile("" ::: "memory"); __builtin_amdgcn_s_barrier(); asm volatile("" ::: "memory"); } while (0)
#define WAITV(n) asm volatile("s_waitcnt vmcnt(%0)" :: "i"(n) : "memory")
#define SB0() __builtin_amdgcn_sched_barrier(0)
#define WAITL0() do { asm volatile("" ::: "memory"); SB0(); __builtin_amdgcn_s_waitcnt(0xC07F); SB0(); asm volatile("" ::: "memory"); } while (0)
#define PAUX 16
#define WOEARLY 1
#define RAUX 16
#define FL7 0
#define PRE 2
#define RD1 0
#define KVAUX 2

constexpr int LDS_X = 0;
constexpr int LDS_K = 32768;
constexpr int LDS_W = 65536;
constexpr int LDS_V = 131072;
constexpr int LDS_TOTAL = 163840;
constexpr int WS_F = 16 << 20;
constexpr int WS_A = WS_F + (256 << 10);
constexpr int WS_BYTES = WS_A + 4096;
constexpr unsigned OOB = 0x7ffffff0u;
constexpr int SPIN_CAP = 1 << 16;
#define PDELAY 16
#define EARLYREL 0
#define SPF 0
#define OAUX 0
#define LFIRST 0
#define FMODE 2
#define ORD 0
#define STAG 0
#define EARLY 0
#define P7 1
#define CPF 1
#define CPFL 8
#define CODE_BYTES 12288
#define LEXP 1
#define CPFO 0
#define PSLEEP 4
#define PBLK(pb) ((0x41324132u >> (4 * (pb))) & 15)

DI bf16x8 tr_frag(LDSP char* p, int off_lo, int off_hi) {
  bf16x4 a = __builtin_amdgcn_ds_read_tr16_b64_v4bf16((LDSP bf16x4*)(p + off_lo));
  bf16x4 b = __builtin_amdgcn_ds_read_tr16_b64_v4bf16((LDSP bf16x4*)(p + off_hi));
  return __builtin_shufflevector(a, b, 0, 1, 2, 3, 4, 5, 6, 7);
}
DI unsigned ld_sc1(__amdgpu_buffer_rsrc_t rs, unsigned ofs) {
  unsigned v;
  asm volatile("buffer_load_dword %0, %1, %2, 0 offen sc1\n\ts_waitcnt vmcnt(0)" : "=&v"(v) : "v"(ofs), "s"(rs) : "memory");
  return v;
}
DI bf16x8 pack8(const f32x16& x, int s) {
  bf16x8 p;
#pragma unroll
  for (int j = 0; j < 8; ++j) p[j] = (__bf16)x[8 * s + j];
  return p;
}

#define TS()
#define TSREC()
#define TSDUMP()
#define CBAR() BAR()
#define LT(k)
#define STAMP(k)
#define RSTAMP(k)
struct FragSet { bf16x8 a[2][4]; bf16x8 b[2]; };

__global__ __launch_bounds__(768, 3) void fused_kernel(const char* __restrict__ x, const char* __restrict__ kc,
                                                       const char* __restrict__ vc, const char* __restrict__ wq,
                                                       const char* __restrict__ wo, char* part, char* out) {
  __shared__ __attribute__((aligned(16))) char smem_[LDS_TOTAL];
  LDSP char* smem = (LDSP char*)smem_;
  const unsigned long long pc0 = __builtin_amdgcn_s_getpc() & ~255ull;
  const int wid0 = __builtin_amdgcn_readfirstlane(threadIdx.x >> 6);
  const int wid = wid0 < 4 ? wid0 : wid0 - 12;
  const int lane = threadIdx.x & 63;
  const int bid = blockIdx.x;
  const int hp = (bid & 7) * 2 + ((bid >> 3) & 1);
  const int b = bid >> 4;
  const int grpB = b & 1;
  const int u0 = b >> 1;
  const int swp = b & 1;

  if (__builtin_expect(wid == 1, 1)) {
    const auto rc = __builtin_amdgcn_make_buffer_rsrc((void*)pc0, 0, CODE_BYTES, 0x00020000);
    __builtin_amdgcn_raw_ptr_buffer_load_lds(rc, (LDSP unsigned*)(smem + LDS_X + 28672), 4, lane < CPFL ? (unsigned)((((bid >> 3) * CPFL + lane + CPFO) & 255) * 64) : OOB, 0, 0, 0);
  }
  asm volatile("" ::: "memory");
  const int lw = wid + 8;
    const int hi = lane >> 5;
    const auto rq = __builtin_amdgcn_make_buffer_rsrc((void*)wq, 0, 4 << 20, 0x00020000);
    const auto ro = __builtin_amdgcn_make_buffer_rsrc((void*)wo, 0, 4 << 20, 0x00020000);
    const auto rx = __builtin_amdgcn_make_buffer_rsrc((void*)x, 0, 1 << 20, 0x00020000);
    const auto rk = __builtin_amdgcn_make_buffer_rsrc((void*)kc, 0, 8 << 20, 0x00020000);
    const auto rv = __builtin_amdgcn_make_buffer_rsrc((void*)vc, 0, 8 << 20, 0x00020000);
    const int wq_e = (4 * lw + hi) * 8192 + hp * 512 + (((lane & 31) ^ (hi << 2)) << 4);
    const int wq_o = (4 * lw + 2 + hi) * 8192 + hp * 512 + (((lane & 31) ^ ((2 + hi) << 2)) << 4);
    const int xg = b * 65536 + (8 * lw + (lane >> 3)) * 1024 + (((lane & 7) ^ (4 * (lw & 1) + (lane >> 4))) << 4);
#define DMA(rs, vo, so, l, aux) __builtin_amdgcn_raw_ptr_buffer_load_lds(rs, (LDSP unsigned*)(l), 16, vo, so, 0, aux)
#define DMA4(rs, vo, so, l) __builtin_amdgcn_raw_ptr_buffer_load_lds(rs, (LDSP unsigned*)(l), 4, vo, so, 0, 0)
    auto issue_pair = [&](int j, int blkofs) {
      const int ku = (u0 + j) & 7;
      LDSP char* d = smem + blkofs + lw * 2048;
#pragma unroll
      for (int sg = 0; sg < 2; ++sg) {
        const int go = (2 * ku + (sg ^ swp)) * 32 * 8192;
        DMA(rq, wq_e, go, d + sg * 16384, 0);
        DMA(rq, wq_o, go, d + sg * 16384 + 1024, 0);
        if (sg == 0) DMA(rx, xg, ku * 128, smem + LDS_X + (j & 3) * 8192 + lw * 1024, 0);
      }
    };
    if (__builtin_expect(wid < 0, 1)) issue_pair(0, LDS_W);
  asm volatile("" ::: "memory");
  if (wid < 0) {
    LT(0);
    const int c16 = ((lane & 15) ^ ((lane >> 4) << 2)) << 4;
    const int wog = (hp * 256 + 32 * lw + (lane >> 3)) * 1024 + (((lane & 7) ^ (((lane >> 4) & 1) << 2)) << 4);
    const int kvb = (b * 32 + hp * 2) * 16384;
    const int vg = kvb + (16 * lw + (lane >> 4)) * 256 + c16;
    const int kg = kvb + (4 * lw + (lane >> 4)) * 256 + (((lane & 15) ^ (4 * (lw & 3) + (lane >> 4))) << 4);
    auto issue_k = [&]() {
#pragma unroll
      for (int i = 0; i < 4; ++i) DMA(rk, kg, i * 8192, smem + LDS_K + i * 8192 + lw * 1024, KVAUX);
    };
    auto issue_v = [&]() {
#pragma unroll
      for (int i = 0; i < 4; ++i) DMA(rv, vg, i * 1024, smem + LDS_V + lw * 4096 + i * 1024, KVAUX);
    };
    auto issue_wo = [&](int c) {
      LDSP char* d = smem + PBLK(c) * 32768 + lw * 4096;
#pragma unroll
      for (int i = 0; i < 4; ++i) DMA(ro, wog, c * 128 + i * 8192, d + i * 1024, 0);
    };
    int pfo = grpB ? (hp * 256 + (b >> 1) * 32 + lw * 4) * 1024 + lane * 64
                   : b * 65536 + ((hp & 1) * 32 + lw * 4) * 1024 + lane * 64;
    asm volatile("" : "+v"(pfo));
    if (!grpB) {
      DMA4(rx, pfo, 65536, smem + LDS_K + lw * 1024);
      DMA4(rx, pfo, 0, smem + LDS_K + lw * 1024);
      issue_k();
    }
    issue_pair(1, LDS_W + 32768);
    WAITV(5); BAR();
    unsigned nonce;
    asm volatile("ds_read_b32 %0, %1\n\ts_waitcnt lgkmcnt(0)" : "=v"(nonce) : "v"(LDS_X + 24576) : "memory");
    nonce = __builtin_amdgcn_readfirstlane(nonce);
    {
      int bo = LDS_W + 65536;
#pragma unroll 1
      for (int j = 2; j < 8; ++j) {
        issue_pair(j, bo);
        bo = (bo == LDS_W + 65536) ? LDS_W : bo + 32768;
        WAITV(5); BAR();
      }
    }
    if (grpB) {
      DMA4(ro, pfo, 0, smem + LDS_K + lw * 1024);
      issue_k();
      WAITV(5); BAR();
      issue_v();
      BAR();
      WAITV(4); BAR();
    } else {
      issue_v();
      WAITV(4); BAR();
      BAR();
      BAR();
    }
    issue_wo(0);
    WAITV(4); BAR();
    issue_wo(1); issue_wo(2);
    WAITV(8); BAR();
    issue_wo(3);
    WAITV(8); BAR();
    if (lw == 7) *(volatile LDSP int*)(smem + LDS_X + 8192) = 0;
#pragma unroll 1
    for (int c = 4; c < 8; ++c) {
      issue_wo(c);
      WAITV(8); BAR();
    }
    WAITV(4); BAR();
    WAITV(0); WAITL0(); BAR();
    {
      const auto prs = __builtin_amdgcn_make_buffer_rsrc(part, 0, WS_BYTES, 0x00020000);
      const auto rout = __builtin_amdgcn_make_buffer_rsrc(out, 0, 1 << 20, 0x00020000);
      const int fofs = WS_F + ((b * 16 + 2 * lw + (lane & 1)) * 4 + (hp >> 2)) * 256 + (hp & 3) * 4;
      const int inb = ((b * 16 + hp) * 16 + 2 * lw) * 4096;
      u32x4 v[2][4];
      unsigned got = 0;
      __builtin_amdgcn_s_sleep(PDELAY);
#pragma unroll 1
      for (int spin = 0; spin < SPIN_CAP; ++spin) {
        const unsigned f = ld_sc1(prs, (lane < 2 && !((got >> lane) & 1)) ? fofs : OOB);
        const unsigned rdy = (unsigned)__ballot(lane < 2 && f == ((nonce << 4) | 8)) | (spin == SPIN_CAP - 1 ? 3u : 0u);
        const unsigned nw = rdy & ~got;
        if (nw & 1) {
#pragma unroll
          for (int i = 0; i < 4; ++i) v[0][i] = __builtin_amdgcn_raw_buffer_load_b128(prs, lane * 16 + i * 1024, inb, 16);
        }
        if (nw & 2) {
#pragma unroll
          for (int i = 0; i < 4; ++i) v[1][i] = __builtin_amdgcn_raw_buffer_load_b128(prs, lane * 16 + i * 1024, inb + 4096, 16);
        }
        got |= rdy;
        if (got == 3) break;
        __builtin_amdgcn_s_sleep(PSLEEP);
      }
      if (lw == 0) RSTAMP(13);
      if (lw == 7) RSTAMP(15);
      LDSP f32x4* slab = (LDSP f32x4*)(smem + LDS_W + lw * 8192) + lane;
#pragma unroll
      for (int i = 0; i < 4; ++i)
#pragma unroll
        for (int e = 0; e < 2; ++e) {
          f32x4 t;
          t[0] = __uint_as_float(v[0][i][2 * e] << 16) + __uint_as_float(v[1][i][2 * e] << 16);
          t[1] = __uint_as_float(v[0][i][2 * e] & 0xffff0000u) + __uint_as_float(v[1][i][2 * e] & 0xffff0000u);
          t[2] = __uint_as_float(v[0][i][2 * e + 1] << 16) + __uint_as_float(v[1][i][2 * e + 1] << 16);
          t[3] = __uint_as_float(v[0][i][2 * e + 1] & 0xffff0000u) + __uint_as_float(v[1][i][2 * e + 1] & 0xffff0000u);
          slab[(2 * i + e) * 64] = t;
        }
      WAITL0();
      if (lane == 0) __hip_atomic_fetch_add((LDSP int*)(smem + LDS_X + 8192), 1, __ATOMIC_RELAXED, __HIP_MEMORY_SCOPE_WORKGROUP);
#pragma unroll 1
      for (int spin = 0; spin < (SPIN_CAP << 6); ++spin) {
        if (__builtin_amdgcn_readfirstlane(*(volatile LDSP int*)(smem + LDS_X + 8192)) >= 8) break;
        __builtin_amdgcn_s_sleep(1);
      }
      asm volatile("" ::: "memory");
      if (lw == 0) RSTAMP(14);
      LDSP f32x4* src = (LDSP f32x4*)(smem + LDS_W) + lw * 64 + lane;
      f32x4 sacc = src[0];
#pragma unroll
      for (int w = 1; w < 8; ++w) sacc += src[w * 512];
      bf16x4 o;
#pragma unroll
      for (int e = 0; e < 4; ++e) o[e] = (__bf16)sacc[e];
      const int c = 2 * (lw >> 1) + (lane >> 5);
      const int row = b * 64 + 32 * ((hp >> 2) & 1) + 8 * (hp & 3) + (lane & 7);
      const int col = 64 * c + 32 * (hp >> 3) + 16 * ((lane >> 4) & 1) + 8 * ((lane >> 3) & 1) + 4 * (lw & 1);
      __builtin_amdgcn_raw_buffer_store_b64(__builtin_bit_cast(u32x2, o), rout, row * 1024 + col * 2, 0, OAUX);
    }
    return;
  }

  const int h = wid >> 1, qh = wid & 1;
  const int r = lane & 31, hh = lane >> 5;
  const int blk = (lane >> 4) & 1, q4 = (lane >> 2) & 3, p4 = lane & 3;
  const int pp = ((p4 & 1) << 1) | (p4 >> 1);
  const int pir = (r & 19) | ((r & 4) << 1) | ((r & 8) >> 1);
  STAMP(0);
  const auto prs = __builtin_amdgcn_make_buffer_rsrc(part, 0, WS_BYTES, 0x00020000);
  if (wid == 0) {
    const unsigned f = ld_sc1(prs, WS_F + ((b * 16 + (lane & 15)) * 4 + (hp >> 2)) * 256 + (hp & 3) * 4) >> 4;
    unsigned cand = 0;
#pragma unroll 1
    while (__any(f == cand)) ++cand;
    __builtin_amdgcn_raw_buffer_store_b32(cand, prs, lane == 0 ? WS_A + b * 256 + hp * 4 : OOB, 0, 16);
    *(LDSP unsigned*)(smem + LDS_X + 24576) = cand;
  }
  WAITL0();

  int woff[4];
#pragma unroll
  for (int dt = 0; dt < 4; ++dt) woff[dt] = LDS_W + (8 * hh + q4) * 512 + h * 256 + ((dt ^ q4) << 6) + blk * 32 + pp * 8;
  const int xrow = 32 * qh + r;
  int xoff[4];
#pragma unroll
  for (int ks = 0; ks < 4; ++ks) xoff[ks] = LDS_X + xrow * 128 + (((2 * (ks ^ (2 * swp)) + hh) ^ ((xrow >> 1) & 7)) << 4);
  int voff[4], koff[8], wa[2];
#pragma unroll
  for (int dt = 0; dt < 4; ++dt) voff[dt] = LDS_V + h * 16384 + (8 * hh + q4) * 256 + ((dt ^ q4) << 6) + blk * 32 + pp * 8;
#pragma unroll
  for (int s = 0; s < 8; ++s) koff[s] = LDS_K + h * 16384 + pir * 256 + (((2 * s + hh) ^ (pir & 15)) << 4);
#pragma unroll
  for (int s = 0; s < 2; ++s) wa[s] = (s ^ h) * 16384 + (8 * hh + q4) * 128 + ((h ^ (q4 >> 1)) << 6) + blk * 32 + pp * 8;
  int ooff = LDS_X + (qh * 16 + h * 8) * 1024 + lane * 16;
  int pofs = ((b * 16 + 4 * wid + (r >> 3)) * 16 + hp) * 4096 + hh * 128 + (r & 7) * 16;
  asm volatile("" : "+v"(voff[0]), "+v"(voff[1]), "+v"(voff[2]), "+v"(voff[3]), "+v"(ooff), "+v"(wa[0]), "+v"(wa[1]), "+v"(pofs));
  asm volatile("" : "+v"(koff[0]), "+v"(koff[1]), "+v"(koff[2]), "+v"(koff[3]), "+v"(koff[4]), "+v"(koff[5]), "+v"(koff[6]), "+v"(koff[7]));
  const f32x16 zero16 = {0.f, 0.f, 0.f, 0.f, 0.f, 0.f, 0.f, 0.f, 0.f, 0.f, 0.f, 0.f, 0.f, 0.f, 0.f, 0.f};
  f32x16 accq[4] = {zero16, zero16, zero16, zero16};

  auto read_stage = [&](FragSet& f, int slot, int xu, int par) {
    LDSP char* wb = smem + slot * 16384;
    LDSP char* xb = smem + xu * 8192;
#pragma unroll
    for (int ks = 0; ks < 2; ++ks) {
      f.b[ks] = *(LDSP bf16x8*)(xb + xoff[2 * par + ks]);
#pragma unroll
      for (int dt = 0; dt < 4; ++dt) f.a[ks][dt] = tr_frag(wb + woff[dt], ks * 8192, ks * 8192 + 2048);
    }
  };
#define MM(c, i) accq[(i) & 3] = MFMA(c.a[(i) >> 2][(i) & 3], c.b[(i) >> 2], accq[(i) & 3])
  auto rd = [&](FragSet& n, LDSP char* wb, LDSP char* xb, int par, int g) {
    if (g == 0) { n.b[0] = *(LDSP bf16x8*)(xb + xoff[2 * par]); n.a[0][0] = tr_frag(wb + woff[0], 0, 2048); }
    if (g == 1) { n.a[0][1] = tr_frag(wb + woff[1], 0, 2048); n.a[0][2] = tr_frag(wb + woff[2], 0, 2048); }
    if (g == 2) { n.a[0][3] = tr_frag(wb + woff[3], 0, 2048); n.b[1] = *(LDSP bf16x8*)(xb + xoff[2 * par + 1]); }
    if (g == 3) { n.a[1][0] = tr_frag(wb + woff[0], 8192, 10240); n.a[1][1] = tr_frag(wb + woff[1], 8192, 10240); }
    if (g == 4) { n.a[1][2] = tr_frag(wb + woff[2], 8192, 10240); n.a[1][3] = tr_frag(wb + woff[3], 8192, 10240); }
  };

  FragSet f0, f1;
  asm volatile("" : "+v"(accq[0]), "+v"(accq[1]), "+v"(accq[2]), "+v"(accq[3]));
  CBAR();
  STAMP(1);
  read_stage(f0, 0, 0, 0);
  const unsigned nonce = __builtin_amdgcn_readfirstlane(*(LDSP unsigned*)(smem + LDS_X + 24576));
  SB0();
#pragma unroll
  for (int i = 0; i < 8; ++i) { MM(f0, i); SB0(); rd(f1, smem + 16384, smem, 1, i); SB0(); }
#pragma unroll
  for (int i = 0; i < PRE; ++i) { MM(f1, i); SB0(); }
  WAITL0();
  {
    int sl = 0;
#pragma unroll 1
    for (int j = 1; j < 8; ++j) {
      CBAR();
      sl = (sl == 4) ? 0 : sl + 2;
      LDSP char* wb = smem + sl * 16384;
      LDSP char* xb = smem + (j & 3) * 8192;
      rd(f0, wb, xb, 0, 0); SB0();
#pragma unroll
      for (int i = PRE; i < 8; ++i) { MM(f1, i); SB0(); rd(f0, wb, xb, 0, i - PRE + 1); SB0(); }
#pragma unroll
      for (int i = 0; i < 8; ++i) { MM(f0, i); SB0(); rd(f1, wb + 16384, xb, 1, i - RD1); SB0(); }
#pragma unroll
      for (int i = 0; i < PRE; ++i) { MM(f1, i); SB0(); }
      WAITL0();
    }
  }
  CBAR();
  STAMP(12);
  bf16x8 kf[2][8], qf[8], vf[4][4], pf[4];
  f32x16 accs[2], acco[4];
#define QM(dt, ks) do { if ((ks) || (dt) >= PRE) accq[dt] = MFMA(f1.a[ks][dt], f1.b[ks], accq[dt]); } while (0)
#define QP(i) qf[i] = pack8(accq[(i) >> 1], (i) & 1)
#define KR(kt, s) kf[kt][s] = *(LDSP bf16x8*)(smem + koff[s] + (kt) * 8192)
#define SM(kt, s) accs[kt] = MFMA(kf[kt][s], qf[s], (s) == 0 ? zero16 : accs[kt])
#define VR(dt, s) vf[dt][s] = tr_frag(smem + voff[dt], (s) * 4096, (s) * 4096 + 1024)
  const unsigned aofs = lane < 4 ? WS_A + b * 256 + (4 * wid + lane) * 4 : OOB;
  unsigned an = __builtin_amdgcn_raw_buffer_load_b32(prs, aofs, 0, 16);
  QM(0, 0); QM(0, 1); QM(1, 0); QM(1, 1); SB0();
  QP(0); SB0(); QM(2, 0); SB0();
  QP(1); SB0(); QM(2, 1); SB0();
  CBAR();
  KR(0, 0); KR(0, 1); KR(0, 2); KR(0, 3); SB0();
  QP(2); SB0(); QM(3, 0); SB0();
  KR(0, 4); KR(0, 5); KR(0, 6); KR(0, 7); SB0();
  QP(3); SB0(); QM(3, 1); SB0();
  QP(4); SB0(); SM(0, 0); SB0();
  QP(5); KR(1, 0); KR(1, 1); SB0(); SM(0, 1); SB0();
  QP(6); KR(1, 2); KR(1, 3); SB0(); SM(0, 2); SB0();
  QP(7); KR(1, 4); KR(1, 5); SB0(); SM(0, 3); SB0();
  KR(1, 6); KR(1, 7); SB0(); SM(0, 4); SM(0, 5); SM(0, 6); SM(0, 7); SB0(); SM(1, 0); SM(1, 1); SM(1, 2); SM(1, 3); SB0();
  float mx = fmaxf(fmaxf(accs[0][0], accs[0][1]), accs[0][2]);
  mx = fmaxf(fmaxf(mx, accs[0][3]), accs[0][4]); mx = fmaxf(fmaxf(mx, accs[0][5]), accs[0][6]);
  asm volatile("" : "+v"(mx)); SB0(); SM(1, 4); SB0();
  mx = fmaxf(fmaxf(mx, accs[0][7]), accs[0][8]); mx = fmaxf(fmaxf(mx, accs[0][9]), accs[0][10]); mx = fmaxf(fmaxf(mx, accs[0][11]), accs[0][12]);
  asm volatile("" : "+v"(mx)); SB0(); SM(1, 5); SB0();
  mx = fmaxf(fmaxf(mx, accs[0][13]), accs[0][14]); mx = fmaxf(mx, accs[0][15]);
  asm volatile("" : "+v"(mx)); SB0(); SM(1, 6); SM(1, 7); SB0();
  STAMP(2);
  {
    float m2 = fmaxf(fmaxf(accs[1][0], accs[1][1]), accs[1][2]);
#pragma unroll
    for (int i = 3; i < 9; i += 2) m2 = fmaxf(fmaxf(m2, accs[1][i]), accs[1][i + 1]);
#pragma unroll
    for (int i = 9; i < 15; i += 2) mx = fmaxf(fmaxf(mx, accs[1][i]), accs[1][i + 1]);
    mx = fmaxf(fmaxf(mx, m2), accs[1][15]);
    auto sw = __builtin_amdgcn_permlane32_swap(__float_as_uint(mx), __float_as_uint(mx), false, false);
    mx = fmaxf(__uint_as_float(sw[0]), __uint_as_float(sw[1]));
  }
  const float sc = 0.08838834764831845f * 1.4426950408889634f;
  const float mb = mx * sc;
  f32x2 sum2 = {0.f, 0.f}, sum3 = {0.f, 0.f};
#define EX(kt, i) do { f32x2 t = {accs[kt][i], accs[kt][(i) + 1]}; t = t * sc - mb; \
    t[0] = __builtin_amdgcn_exp2f(t[0]); t[1] = __builtin_amdgcn_exp2f(t[1]); \
    accs[kt][i] = t[0]; accs[kt][(i) + 1] = t[1]; if ((i) & 2) sum3 += t; else sum2 += t; } while (0)
#pragma unroll
  for (int i = 0; i < 16; i += 2) EX(0, i);
  WAITL0();
  CBAR();
#pragma unroll
  for (int i = 0; i < 4; ++i) { VR(0, i); VR(1, i); VR(2, i); VR(3, i); SB0(); EX(1, 4 * i); EX(1, 4 * i + 2); SB0(); }
  sum2 += sum3;
  float sum = sum2[0] + sum2[1];
  {
    auto sw = __builtin_amdgcn_permlane32_swap(__float_as_uint(sum), __float_as_uint(sum), false, false);
    sum = __uint_as_float(sw[0]) + __uint_as_float(sw[1]);
  }
  const float inv = __builtin_amdgcn_rcpf(sum);
  SB0();
  bf16x8 of[16], wf[2][8];
  f32x16 acc[2];
#define PH(s, hf) do { _Pragma("unroll") for (int j = 4 * (hf); j < 4 * (hf) + 4; j += 2) { \
    f32x2 t = {accs[(s) >> 1][8 * ((s) & 1) + j], accs[(s) >> 1][8 * ((s) & 1) + j + 1]}; t *= inv; \
    pf[s][j] = (__bf16)t[0]; pf[s][j + 1] = (__bf16)t[1]; } } while (0)
#define OM(dt, s) acco[dt] = MFMA(vf[dt][s], pf[s], (s) == 0 ? zero16 : acco[dt])
#define OW(i) do { of[i] = pack8(acco[(i) >> 1], (i) & 1); *(LDSP bf16x8*)(smem + ooff + (i) * 1024) = of[i]; } while (0)
  PH(0, 0); PH(0, 1);
  asm volatile("" : "+v"(an));
  SB0();
  OM(0, 0); SB0(); PH(1, 0); SB0();
  OM(1, 0); SB0(); PH(1, 1); SB0();
  OM(2, 0); SB0(); PH(2, 0); SB0();
  OM(3, 0); SB0(); PH(2, 1); SB0();
  OM(0, 1); SB0(); PH(3, 0); SB0();
  OM(1, 1); SB0(); PH(3, 1); SB0();
  OM(2, 1); SB0(); OM(3, 1); SB0(); OM(0, 2); SB0(); OM(0, 3); SB0(); OM(1, 2); SB0(); OM(1, 3); SB0();
  OW(0); SB0(); OM(2, 2); SB0();
  OW(1); SB0(); OM(2, 3); SB0();
  OW(2); SB0(); OM(3, 2); SB0();
  OW(3); SB0(); OM(3, 3); SB0();
  OW(4); OW(5); SB0(); OW(6); OW(7);
  WAITL0();
  STAMP(3);
  CBAR();
  STAMP(4);
#define WR(u, i) wf[(u) & 1][i] = tr_frag(wp, (i) * 2048, (i) * 2048 + 512)
#define PM(u, i) acc[((u) >> 1) & 1] = MFMA(wf[(u) & 1][i], of[8 * ((u) & 1) + (i)], (((u) & 1) == 0 && (i) == 0) ? zero16 : acc[((u) >> 1) & 1])
#define PST(c, a) __builtin_amdgcn_raw_buffer_store_b128(__builtin_bit_cast(u32x4, pack8(acc[(c) & 1], a)), prs, pofs + (c) * 512 + (a) * 256, 0, PAUX)
  const unsigned fofs = lane < 4 ? WS_F + ((b * 16 + hp) * 4 + wid) * 256 + lane * 4 : OOB;
#define FLAG(n) __builtin_amdgcn_raw_buffer_store_b32((an << 4) | (n), prs, fofs, 0, 16)
  LDSP char* wp = smem + wa[0] + PBLK(0) * 32768;
  bf16x8 pk[2];
#pragma unroll
  for (int i = 0; i < 8; ++i) WR(0, i);
  SB0();
#pragma unroll
  for (int u = 0; u < 16; ++u) {
    if ((u & 1) && u < (P7 ? 15 : 13)) { WAITL0(); CBAR(); }
    if (u < 15) { int wo_ = wa[(u + 1) & 1] + PBLK((u + 1) >> 1) * 32768; asm volatile("" : "+v"(wo_)); wp = smem + wo_; }
    const int c = (u >> 1) - 1;
#pragma unroll
    for (int i = 0; i < 8; ++i) {
      PM(u, i); SB0();
      if (u == 0) { if (i < 4) { of[8 + 2 * i] = *(LDSP bf16x8*)(smem + (ooff ^ 8192) + 2 * i * 1024); of[9 + 2 * i] = *(LDSP bf16x8*)(smem + (ooff ^ 8192) + (2 * i + 1) * 1024); } }
      if (u < 15) { if (i < 2) { WR(u + 1, 2 * i); WR(u + 1, 2 * i + 1); } else if (i < 6) WR(u + 1, i + 2); }
      if ((u & 1) == 0 && u > 0) {
        if (i == 4) PST(c, 0);
        if (i == 5) PST(c, 1);
      }
      SB0();
    }
    if ((u & 3) == 3) STAMP(5 + (u >> 2));
  }
  PST(7, 0); PST(7, 1);
  WAITV(0);
#if !(PAUX & 16)
  __builtin_amdgcn_fence(__ATOMIC_RELEASE, "agent");
  WAITV(0);
#endif
  FLAG(8);
  STAMP(9);
  {
    const unsigned vofs = lane < 4 ? WS_F + ((b * 16 + 4 * wid + lane) * 4 + (hp >> 2)) * 256 + (hp & 3) * 4 : OOB;
#pragma unroll 1
    for (int spin = 0; spin < SPIN_CAP; ++spin) {
      const unsigned f = ld_sc1(prs, vofs);
      const bool ok = __all(lane >= 4 || (f >> 4) == nonce);
      const unsigned a2 = ld_sc1(prs, aofs);
      if (__any(a2 != an)) { an = a2; FLAG(8); }
      if (ok) break;
      __builtin_amdgcn_s_sleep(8);
    }
  }
}

extern "C" void kernel_launch(void* const* d_in, const int* in_sizes, int n_in, void* d_out, int out_size, void* d_ws,
                              size_t ws_size, hipStream_t stream) {
  fused_kernel<<<256, 768, 0, stream>>>((const char*)d_in[0], (const char*)d_in[1], (const char*)d_in[2],
                                        (const char*)d_in[3], (const char*)d_in[4], (char*)d_ws, (char*)d_out);
}
```

```cpp
#include <hip/hip_runtime.h>
#include <hip/hip_bf16.h>

typedef __bf16 bf16x4 __attribute__((ext_vector_type(4)));
typedef __bf16 bf16x8 __attribute__((ext_vector_type(8)));
typedef float f32x16 __attribute__((ext_vector_type(16)));
typedef float f32x2 __attribute__((ext_vector_type(2)));
typedef unsigned u32x4 __attribute__((ext_vector_type(4)));
typedef unsigned u32x2 __attribute__((ext_vector_type(2)));
typedef float f32x4 __attribute__((ext_vector_type(4)));
#define LDSP __attribute__((address_space(3)))
#define DI __device__ __forceinline__
#define MFMA(a, b, c) __builtin_amdgcn_mfma_f32_32x32x16_bf16((a), (b), (c), 0, 0, 0)
#define BAR() do { asm volatile("" ::: "memory"); __builtin_amdgcn_s_barrier(); asm volatile("" ::: "memory"); } while (0)
#define WAITV(n) asm volatile("s_waitcnt vmcnt(%0)" :: "i"(n) : "memory")
#define SB0() __builtin_amdgcn_sched_barrier(0)
#define WAITL0() do { asm volatile("" ::: "memory"); SB0(); __builtin_amdgcn_s_waitcnt(0xC07F); SB0(); asm volatile("" ::: "memory"); } while (0)
#define PAUX 16
#define WOEARLY 1
#define RAUX 16
#define FL7 0
#define PRE 2
#define RD1 0
#define KVAUX 2

constexpr int LDS_X = 0;
constexpr int LDS_K = 32768;
constexpr int LDS_W = 65536;
constexpr int LDS_V = 131072;
constexpr int LDS_TOTAL = 163840;
constexpr int WS_F = 16 << 20;
constexpr int WS_A = WS_F + (256 << 10);
constexpr int WS_BYTES = WS_A + 4096;
constexpr unsigned OOB = 0x7ffffff0u;
constexpr int SPIN_CAP = 1 << 16;
#define PDELAY 16
#define EARLYREL 0
#define SPF 0
#define OAUX 0
#define LFIRST 0
#define FMODE 2
#define ORD 0
#define STAG 0
#define EARLY 0
#define P7 1
#define CPF 1
#define CPFL 8
#define CODE_BYTES 12288
#define LEXP 1
#define CPFO 0
#define TPF 3
#define TSLEEP 16
#define PSLEEP 4
#define PBLK(pb) ((0x41324132u >> (4 * (pb))) & 15)

DI bf16x8 tr_frag(LDSP char* p, int off_lo, int off_hi) {
  bf16x4 a = __builtin_amdgcn_ds_read_tr16_b64_v4bf16((LDSP bf16x4*)(p + off_lo));
  bf16x4 b = __builtin_amdgcn_ds_read_tr16_b64_v4bf16((LDSP bf16x4*)(p + off_hi));
  return __builtin_shufflevector(a, b, 0, 1, 2, 3, 4, 5, 6, 7);
}
DI unsigned ld_sc1(__amdgpu_buffer_rsrc_t rs, unsigned ofs) {
  unsigned v;
  asm volatile("buffer_load_dword %0, %1, %2, 0 offen sc1\n\ts_waitcnt vmcnt(0)" : "=&v"(v) : "v"(ofs), "s"(rs) : "memory");
  return v;
}
DI bf16x8 pack8(const f32x16& x, int s) {
  bf16x8 p;
#pragma unroll
  for (int j = 0; j < 8; ++j) p[j] = (__bf16)x[8 * s + j];
  return p;
}

#define TS()
#define TSREC()
#define TSDUMP()
#define CBAR() BAR()
#define LT(k)
#define STAMP(k)
#define RSTAMP(k)
struct FragSet { bf16x8 a[2][4]; bf16x8 b[2]; };

__global__ __launch_bounds__(768, 3) void fused_kernel(const char* __restrict__ x, const char* __restrict__ kc,
                                                       const char* __restrict__ vc, const char* __restrict__ wq,
                                                       const char* __restrict__ wo, char* part, char* out) {
  __shared__ __attribute__((aligned(16))) char smem_[LDS_TOTAL];
  LDSP char* smem = (LDSP char*)smem_;
  const unsigned long long pc0 = __builtin_amdgcn_s_getpc() & ~255ull;
  const int wid0 = __builtin_amdgcn_readfirstlane(threadIdx.x >> 6);
  const int wid = wid0 < 4 ? wid0 : wid0 - 12;
  const int lane = threadIdx.x & 63;
  const int bid = blockIdx.x;
  const int hp = (bid & 7) * 2 + ((bid >> 3) & 1);
  const int b = bid >> 4;
  const int grpB = b & 1;
  const int u0 = b >> 1;
  const int swp = b & 1;

  if (__builtin_expect(wid == 1, 1)) {
    const auto rc = __builtin_amdgcn_make_buffer_rsrc((void*)pc0, 0, CODE_BYTES, 0x00020000);
    __builtin_amdgcn_raw_ptr_buffer_load_lds(rc, (LDSP unsigned*)(smem + LDS_X + 28672), 4, lane < CPFL ? (unsigned)((((bid >> 3) * CPFL + lane + CPFO) & 255) * 64) : OOB, 0, 0, 0);
  }
  asm volatile("" ::: "memory");
  const int lw = wid + 8;
    const int hi = lane >> 5;
    const auto rq = __builtin_amdgcn_make_buffer_rsrc((void*)wq, 0, 4 << 20, 0x00020000);
    const auto ro = __builtin_amdgcn_make_buffer_rsrc((void*)wo, 0, 4 << 20, 0x00020000);
    const auto rx = __builtin_amdgcn_make_buffer_rsrc((void*)x, 0, 1 << 20, 0x00020000);
    const auto rk = __builtin_amdgcn_make_buffer_rsrc((void*)kc, 0, 8 << 20, 0x00020000);
    const auto rv = __builtin_amdgcn_make_buffer_rsrc((void*)vc, 0, 8 << 20, 0x00020000);
    const int wq_e = (4 * lw + hi) * 8192 + hp * 512 + (((lane & 31) ^ (hi << 2)) << 4);
    const int wq_o = (4 * lw + 2 + hi) * 8192 + hp * 512 + (((lane & 31) ^ ((2 + hi) << 2)) << 4);
    const int xg = b * 65536 + (8 * lw + (lane >> 3)) * 1024 + (((lane & 7) ^ (4 * (lw & 1) + (lane >> 4))) << 4);
#define DMA(rs, vo, so, l, aux) __builtin_amdgcn_raw_ptr_buffer_load_lds(rs, (LDSP unsigned*)(l), 16, vo, so, 0, aux)
#define DMA4(rs, vo, so, l) __builtin_amdgcn_raw_ptr_buffer_load_lds(rs, (LDSP unsigned*)(l), 4, vo, so, 0, 0)
    auto issue_pair = [&](int j, int blkofs) {
      const int ku = (u0 + j) & 7;
      LDSP char* d = smem + blkofs + lw * 2048;
#pragma unroll
      for (int sg = 0; sg < 2; ++sg) {
        const int go = (2 * ku + (sg ^ swp)) * 32 * 8192;
        DMA(rq, wq_e, go, d + sg * 16384, 0);
        DMA(rq, wq_o, go, d + sg * 16384 + 1024, 0);
        if (sg == 0) DMA(rx, xg, ku * 128, smem + LDS_X + (j & 3) * 8192 + lw * 1024, 0);
      }
    };
    if (__builtin_expect(wid < 0, 1)) issue_pair(0, LDS_W);
  asm volatile("" ::: "memory");
  if (wid < 0) {
    LT(0);
    const int c16 = ((lane & 15) ^ ((lane >> 4) << 2)) << 4;
    const int wog = (hp * 256 + 32 * lw + (lane >> 3)) * 1024 + (((lane & 7) ^ (((lane >> 4) & 1) << 2)) << 4);
    const int kvb = (b * 32 + hp * 2) * 16384;
    const int vg = kvb + (16 * lw + (lane >> 4)) * 256 + c16;
    const int kg = kvb + (4 * lw + (lane >> 4)) * 256 + (((lane & 15) ^ (4 * (lw & 3) + (lane >> 4))) << 4);
    auto issue_k = [&]() {
#pragma unroll
      for (int i = 0; i < 4; ++i) DMA(rk, kg, i * 8192, smem + LDS_K + i * 8192 + lw * 1024, KVAUX);
    };
    auto issue_v = [&]() {
#pragma unroll
      for (int i = 0; i < 4; ++i) DMA(rv, vg, i * 1024, smem + LDS_V + lw * 4096 + i * 1024, KVAUX);
    };
    auto issue_wo = [&](int c) {
      LDSP char* d = smem + PBLK(c) * 32768 + lw * 4096;
#pragma unroll
      for (int i = 0; i < 4; ++i) DMA(ro, wog, c * 128 + i * 8192, d + i * 1024, 0);
    };
    int pfo = grpB ? (hp * 256 + (b >> 1) * 32 + lw * 4) * 1024 + lane * 64
                   : b * 65536 + ((hp & 1) * 32 + lw * 4) * 1024 + lane * 64;
    asm volatile("" : "+v"(pfo));
    if (!grpB) {
      DMA4(rx, pfo, 65536, smem + LDS_K + lw * 1024);
      DMA4(rx, pfo, 0, smem + LDS_K + lw * 1024);
      issue_k();
    }
    issue_pair(1, LDS_W + 32768);
    WAITV(5); BAR();
    unsigned nonce;
    asm volatile("ds_read_b32 %0, %1\n\ts_waitcnt lgkmcnt(0)" : "=v"(nonce) : "v"(LDS_X + 24576) : "memory");
    nonce = __builtin_amdgcn_readfirstlane(nonce);
    {
      int bo = LDS_W + 65536;
#pragma unroll 1
      for (int j = 2; j < 8; ++j) {
        issue_pair(j, bo);
        bo = (bo == LDS_W + 65536) ? LDS_W : bo + 32768;
        WAITV(5); BAR();
      }
    }
    if (grpB) {
      DMA4(ro, pfo, 0, smem + LDS_K + lw * 1024);
      issue_k();
      WAITV(5); BAR();
      issue_v();
      BAR();
      WAITV(4); BAR();
    } else {
      issue_v();
      WAITV(4); BAR();
      BAR();
      BAR();
    }
    issue_wo(0);
    WAITV(4); BAR();
    issue_wo(1); issue_wo(2);
    WAITV(8); BAR();
    issue_wo(3);
    WAITV(8); BAR();
    if (lw == 7) *(volatile LDSP int*)(smem + LDS_X + 8192) = 0;
#pragma unroll 1
    for (int c = 4; c < 8; ++c) {
      issue_wo(c);
      WAITV(8); BAR();
    }
    WAITV(4); BAR();
    WAITV(0); WAITL0(); BAR();
    {
      const auto prs = __builtin_amdgcn_make_buffer_rsrc(part, 0, WS_BYTES, 0x00020000);
      const auto rout = __builtin_amdgcn_make_buffer_rsrc(out, 0, 1 << 20, 0x00020000);
      const int fofs = WS_F + ((b * 16 + 2 * lw + (lane & 1)) * 4 + (hp >> 2)) * 256 + (hp & 3) * 4;
      const int inb = ((b * 16 + hp) * 16 + 2 * lw) * 4096;
      u32x4 v[2][4];
      unsigned got = 0;
      __builtin_amdgcn_s_sleep(PDELAY);
#pragma unroll 1
      for (int spin = 0; spin < SPIN_CAP; ++spin) {
        const unsigned f = ld_sc1(prs, (lane < 2 && !((got >> lane) & 1)) ? fofs : OOB);
        const unsigned rdy = (unsigned)__ballot(lane < 2 && f == ((nonce << 4) | 8)) | (spin == SPIN_CAP - 1 ? 3u : 0u);
        const unsigned nw = rdy & ~got;
        if (nw & 1) {
#pragma unroll
          for (int i = 0; i < 4; ++i) v[0][i] = __builtin_amdgcn_raw_buffer_load_b128(prs, lane * 16 + i * 1024, inb, 16);
        }
        if (nw & 2) {
#pragma unroll
          for (int i = 0; i < 4; ++i) v[1][i] = __builtin_amdgcn_raw_buffer_load_b128(prs, lane * 16 + i * 1024, inb + 4096, 16);
        }
        got |= rdy;
        if (got == 3) break;
        __builtin_amdgcn_s_sleep(PSLEEP);
      }
      if (lw == 0) RSTAMP(13);
      if (lw == 7) RSTAMP(15);
      LDSP f32x4* slab = (LDSP f32x4*)(smem + LDS_W + lw * 8192) + lane;
#pragma unroll
      for (int i = 0; i < 4; ++i)
#pragma unroll
        for (int e = 0; e < 2; ++e) {
          f32x4 t;
          t[0] = __uint_as_float(v[0][i][2 * e] << 16) + __uint_as_float(v[1][i][2 * e] << 16);
          t[1] = __uint_as_float(v[0][i][2 * e] & 0xffff0000u) + __uint_as_float(v[1][i][2 * e] & 0xffff0000u);
          t[2] = __uint_as_float(v[0][i][2 * e + 1] << 16) + __uint_as_float(v[1][i][2 * e + 1] << 16);
          t[3] = __uint_as_float(v[0][i][2 * e + 1] & 0xffff0000u) + __uint_as_float(v[1][i][2 * e + 1] & 0xffff0000u);
          slab[(2 * i + e) * 64] = t;
        }
      WAITL0();
      if (lane == 0) __hip_atomic_fetch_add((LDSP int*)(smem + LDS_X + 8192), 1, __ATOMIC_RELAXED, __HIP_MEMORY_SCOPE_WORKGROUP);
#pragma unroll 1
      for (int spin = 0; spin < (SPIN_CAP << 6); ++spin) {
        if (__builtin_amdgcn_readfirstlane(*(volatile LDSP int*)(smem + LDS_X + 8192)) >= 8) break;
        __builtin_amdgcn_s_sleep(1);
      }
      asm volatile("" ::: "memory");
      if (lw == 0) RSTAMP(14);
      LDSP f32x4* src = (LDSP f32x4*)(smem + LDS_W) + lw * 64 + lane;
      f32x4 sacc = src[0];
#pragma unroll
      for (int w = 1; w < 8; ++w) sacc += src[w * 512];
      bf16x4 o;
#pragma unroll
      for (int e = 0; e < 4; ++e) o[e] = (__bf16)sacc[e];
      const int c = 2 * (lw >> 1) + (lane >> 5);
      const int row = b * 64 + 32 * ((hp >> 2) & 1) + 8 * (hp & 3) + (lane & 7);
      const int col = 64 * c + 32 * (hp >> 3) + 16 * ((lane >> 4) & 1) + 8 * ((lane >> 3) & 1) + 4 * (lw & 1);
      __builtin_amdgcn_raw_buffer_store_b64(__builtin_bit_cast(u32x2, o), rout, row * 1024 + col * 2, 0, OAUX);
    }
    return;
  }

  const int h = wid >> 1, qh = wid & 1;
  const int r = lane & 31, hh = lane >> 5;
  const int blk = (lane >> 4) & 1, q4 = (lane >> 2) & 3, p4 = lane & 3;
  const int pp = ((p4 & 1) << 1) | (p4 >> 1);
  const int pir = (r & 19) | ((r & 4) << 1) | ((r & 8) >> 1);
  STAMP(0);
  if (wid == 2 && !grpB) {
    __builtin_amdgcn_s_sleep(TSLEEP);
    unsigned d0, d1, d2, d3, d4;
    asm volatile("s_load_dword %0, %5, 0\n\ts_load_dword %1, %6, 0\n\ts_load_dword %2, %7, 0\n\ts_load_dword %3, %8, 0\n\ts_load_dword %4, %9, 0\n\ts_waitcnt lgkmcnt(0)"
                 : "=&s"(d0), "=&s"(d1), "=&s"(d2), "=&s"(d3), "=&s"(d4)
                 : "s"(((TPF & 2) ? kc : vc) + (size_t)(b * 32 + hp * 2) * 16384), "s"(vc + (size_t)(b * 32 + hp * 2) * 16384),
                   "s"(wo + (size_t)hp * 262144 + b * 16384), "s"(part + (size_t)(b * 16 + hp) * 65536), "s"(out + (size_t)b * 65536 + hp * 4096)
                 : "memory");
  }
  const auto prs = __builtin_amdgcn_make_buffer_rsrc(part, 0, WS_BYTES, 0x00020000);
  if (wid == 0) {
    const unsigned f = ld_sc1(prs, WS_F + ((b * 16 + (lane & 15)) * 4 + (hp >> 2)) * 256 + (hp & 3) * 4) >> 4;
    unsigned cand = 0;
#pragma unroll 1
    while (__any(f == cand)) ++cand;
    __builtin_amdgcn_raw_buffer_store_b32(cand, prs, lane == 0 ? WS_A + b * 256 + hp * 4 : OOB, 0, 16);
    *(LDSP unsigned*)(smem + LDS_X + 24576) = cand;
  }
  WAITL0();

  int woff[4];
#pragma unroll
  for (int dt = 0; dt < 4; ++dt) woff[dt] = LDS_W + (8 * hh + q4) * 512 + h * 256 + ((dt ^ q4) << 6) + blk * 32 + pp * 8;
  const int xrow = 32 * qh + r;
  int xoff[4];
#pragma unroll
  for (int ks = 0; ks < 4; ++ks) xoff[ks] = LDS_X + xrow * 128 + (((2 * (ks ^ (2 * swp)) + hh) ^ ((xrow >> 1) & 7)) << 4);
  int voff[4], koff[8], wa[2];
#pragma unroll
  for (int dt = 0; dt < 4; ++dt) voff[dt] = LDS_V + h * 16384 + (8 * hh + q4) * 256 + ((dt ^ q4) << 6) + blk * 32 + pp * 8;
#pragma unroll
  for (int s = 0; s < 8; ++s) koff[s] = LDS_K + h * 16384 + pir * 256 + (((2 * s + hh) ^ (pir & 15)) << 4);
#pragma unroll
  for (int s = 0; s < 2; ++s) wa[s] = (s ^ h) * 16384 + (8 * hh + q4) * 128 + ((h ^ (q4 >> 1)) << 6) + blk * 32 + pp * 8;
  int ooff = LDS_X + (qh * 16 + h * 8) * 1024 + lane * 16;
  int pofs = ((b * 16 + 4 * wid + (r >> 3)) * 16 + hp) * 4096 + hh * 128 + (r & 7) * 16;
  asm volatile("" : "+v"(voff[0]), "+v"(voff[1]), "+v"(voff[2]), "+v"(voff[3]), "+v"(ooff), "+v"(wa[0]), "+v"(wa[1]), "+v"(pofs));
  asm volatile("" : "+v"(koff[0]), "+v"(koff[1]), "+v"(koff[2]), "+v"(koff[3]), "+v"(koff[4]), "+v"(koff[5]), "+v"(koff[6]), "+v"(koff[7]));
  const f32x16 zero16 = {0.f, 0.f, 0.f, 0.f, 0.f, 0.f, 0.f, 0.f, 0.f, 0.f, 0.f, 0.f, 0.f, 0.f, 0.f, 0.f};
  f32x16 accq[4] = {zero16, zero16, zero16, zero16};

  auto read_stage = [&](FragSet& f, int slot, int xu, int par) {
    LDSP char* wb = smem + slot * 16384;
    LDSP char* xb = smem + xu * 8192;
#pragma unroll
    for (int ks = 0; ks < 2; ++ks) {
      f.b[ks] = *(LDSP bf16x8*)(xb + xoff[2 * par + ks]);
#pragma unroll
      for (int dt = 0; dt < 4; ++dt) f.a[ks][dt] = tr_frag(wb + woff[dt], ks * 8192, ks * 8192 + 2048);
    }
  };
#define MM(c, i) accq[(i) & 3] = MFMA(c.a[(i) >> 2][(i) & 3], c.b[(i) >> 2], accq[(i) & 3])
  auto rd = [&](FragSet& n, LDSP char* wb, LDSP char* xb, int par, int g) {
    if (g == 0) { n.b[0] = *(LDSP bf16x8*)(xb + xoff[2 * par]); n.a[0][0] = tr_frag(wb + woff[0], 0, 2048); }
    if (g == 1) { n.a[0][1] = tr_frag(wb + woff[1], 0, 2048); n.a[0][2] = tr_frag(wb + woff[2], 0, 2048); }
    if (g == 2) { n.a[0][3] = tr_frag(wb + woff[3], 0, 2048); n.b[1] = *(LDSP bf16x8*)(xb + xoff[2 * par + 1]); }
    if (g == 3) { n.a[1][0] = tr_frag(wb + woff[0], 8192, 10240); n.a[1][1] = tr_frag(wb + woff[1], 8192, 10240); }
    if (g == 4) { n.a[1][2] = tr_frag(wb + woff[2], 8192, 10240); n.a[1][3] = tr_frag(wb + woff[3], 8192, 10240); }
  };

  FragSet f0, f1;
  asm volatile("" : "+v"(accq[0]), "+v"(accq[1]), "+v"(accq[2]), "+v"(accq[3]));
  CBAR();
  STAMP(1);
  read_stage(f0, 0, 0, 0);
  const unsigned nonce = __builtin_amdgcn_readfirstlane(*(LDSP unsigned*)(smem + LDS_X + 24576));
  SB0();
#pragma unroll
  for (int i = 0; i < 8; ++i) { MM(f0, i); SB0(); rd(f1, smem + 16384, smem, 1, i); SB0(); }
#pragma unroll
  for (int i = 0; i < PRE; ++i) { MM(f1, i); SB0(); }
  WAITL0();
  {
    int sl = 0;
#pragma unroll 1
    for (int j = 1; j < 8; ++j) {
      CBAR();
      sl = (sl == 4) ? 0 : sl + 2;
      LDSP char* wb = smem + sl * 16384;
      LDSP char* xb = smem + (j & 3) * 8192;
      rd(f0, wb, xb, 0, 0); SB0();
#pragma unroll
      for (int i = PRE; i < 8; ++i) { MM(f1, i); SB0(); rd(f0, wb, xb, 0, i - PRE + 1); SB0(); }
#pragma unroll
      for (int i = 0; i < 8; ++i) { MM(f0, i); SB0(); rd(f1, wb + 16384, xb, 1, i - RD1); SB0(); }
#pragma unroll
      for (int i = 0; i < PRE; ++i) { MM(f1, i); SB0(); }
      WAITL0();
    }
  }
  CBAR();
  STAMP(12);
  bf16x8 kf[2][8], qf[8], vf[4][4], pf[4];
  f32x16 accs[2], acco[4];
#define QM(dt, ks) do { if ((ks) || (dt) >= PRE) accq[dt] = MFMA(f1.a[ks][dt], f1.b[ks], accq[dt]); } while (0)
#define QP(i) qf[i] = pack8(accq[(i) >> 1], (i) & 1)
#define KR(kt, s) kf[kt][s] = *(LDSP bf16x8*)(smem + koff[s] + (kt) * 8192)
#define SM(kt, s) accs[kt] = MFMA(kf[kt][s], qf[s], (s) == 0 ? zero16 : accs[kt])
#define VR(dt, s) vf[dt][s] = tr_frag(smem + voff[dt], (s) * 4096, (s) * 4096 + 1024)
  const unsigned aofs = lane < 4 ? WS_A + b * 256 + (4 * wid + lane) * 4 : OOB;
  unsigned an = __builtin_amdgcn_raw_buffer_load_b32(prs, aofs, 0, 16);
  QM(0, 0); QM(0, 1); QM(1, 0); QM(1, 1); SB0();
  QP(0); SB0(); QM(2, 0); SB0();
  QP(1); SB0(); QM(2, 1); SB0();
  CBAR();
  KR(0, 0); KR(0, 1); KR(0, 2); KR(0, 3); SB0();
  QP(2); SB0(); QM(3, 0); SB0();
  KR(0, 4); KR(0, 5); KR(0, 6); KR(0, 7); SB0();
  QP(3); SB0(); QM(3, 1); SB0();
  QP(4); SB0(); SM(0, 0); SB0();
  QP(5); KR(1, 0); KR(1, 1); SB0(); SM(0, 1); SB0();
  QP(6); KR(1, 2); KR(1, 3); SB0(); SM(0, 2); SB0();
  QP(7); KR(1, 4); KR(1, 5); SB0(); SM(0, 3); SB0();
  KR(1, 6); KR(1, 7); SB0(); SM(0, 4); SM(0, 5); SM(0, 6); SM(0, 7); SB0(); SM(1, 0); SM(1, 1); SM(1, 2); SM(1, 3); SB0();
  float mx = fmaxf(fmaxf(accs[0][0], accs[0][1]), accs[0][2]);
  mx = fmaxf(fmaxf(mx, accs[0][3]), accs[0][4]); mx = fmaxf(fmaxf(mx, accs[0][5]), accs[0][6]);
  asm volatile("" : "+v"(mx)); SB0(); SM(1, 4); SB0();
  mx = fmaxf(fmaxf(mx, accs[0][7]), accs[0][8]); mx = fmaxf(fmaxf(mx, accs[0][9]), accs[0][10]); mx = fmaxf(fmaxf(mx, accs[0][11]), accs[0][12]);
  asm volatile("" : "+v"(mx)); SB0(); SM(1, 5); SB0();
  mx = fmaxf(fmaxf(mx, accs[0][13]), accs[0][14]); mx = fmaxf(mx, accs[0][15]);
  asm volatile("" : "+v"(mx)); SB0(); SM(1, 6); SM(1, 7); SB0();
  STAMP(2);
  {
    float m2 = fmaxf(fmaxf(accs[1][0], accs[1][1]), accs[1][2]);
#pragma unroll
    for (int i = 3; i < 9; i += 2) m2 = fmaxf(fmaxf(m2, accs[1][i]), accs[1][i + 1]);
#pragma unroll
    for (int i = 9; i < 15; i += 2) mx = fmaxf(fmaxf(mx, accs[1][i]), accs[1][i + 1]);
    mx = fmaxf(fmaxf(mx, m2), accs[1][15]);
    auto sw = __builtin_amdgcn_permlane32_swap(__float_as_uint(mx), __float_as_uint(mx), false, false);
    mx = fmaxf(__uint_as_float(sw[0]), __uint_as_float(sw[1]));
  }
  const float sc = 0.08838834764831845f * 1.4426950408889634f;
  const float mb = mx * sc;
  f32x2 sum2 = {0.f, 0.f}, sum3 = {0.f, 0.f};
#define EX(kt, i) do { f32x2 t = {accs[kt][i], accs[kt][(i) + 1]}; t = t * sc - mb; \
    t[0] = __builtin_amdgcn_exp2f(t[0]); t[1] = __builtin_amdgcn_exp2f(t[1]); \
    accs[kt][i] = t[0]; accs[kt][(i) + 1] = t[1]; if ((i) & 2) sum3 += t; else sum2 += t; } while (0)
#pragma unroll
  for (int i = 0; i < 16; i += 2) EX(0, i);
  WAITL0();
  CBAR();
#pragma unroll
  for (int i = 0; i < 4; ++i) { VR(0, i); VR(1, i); VR(2, i); VR(3, i); SB0(); EX(1, 4 * i); EX(1, 4 * i + 2); SB0(); }
  sum2 += sum3;
  float sum = sum2[0] + sum2[1];
  {
    auto sw = __builtin_amdgcn_permlane32_swap(__float_as_uint(sum), __float_as_uint(sum), false, false);
    sum = __uint_as_float(sw[0]) + __uint_as_float(sw[1]);
  }
  const float inv = __builtin_amdgcn_rcpf(sum);
  SB0();
  bf16x8 of[16], wf[2][8];
  f32x16 acc[2];
#define PH(s, hf) do { _Pragma("unroll") for (int j = 4 * (hf); j < 4 * (hf) + 4; j += 2) { \
    f32x2 t = {accs[(s) >> 1][8 * ((s) & 1) + j], accs[(s) >> 1][8 * ((s) & 1) + j + 1]}; t *= inv; \
    pf[s][j] = (__bf16)t[0]; pf[s][j + 1] = (__bf16)t[1]; } } while (0)
#define OM(dt, s) acco[dt] = MFMA(vf[dt][s], pf[s], (s) == 0 ? zero16 : acco[dt])
#define OW(i) do { of[i] = pack8(acco[(i) >> 1], (i) & 1); *(LDSP bf16x8*)(smem + ooff + (i) * 1024) = of[i]; } while (0)
  PH(0, 0); PH(0, 1);
  asm volatile("" : "+v"(an));
  SB0();
  OM(0, 0); SB0(); PH(1, 0); SB0();
  OM(1, 0); SB0(); PH(1, 1); SB0();
  OM(2, 0); SB0(); PH(2, 0); SB0();
  OM(3, 0); SB0(); PH(2, 1); SB0();
  OM(0, 1); SB0(); PH(3, 0); SB0();
  OM(1, 1); SB0(); PH(3, 1); SB0();
  OM(2, 1); SB0(); OM(3, 1); SB0(); OM(0, 2); SB0(); OM(0, 3); SB0(); OM(1, 2); SB0(); OM(1, 3); SB0();
  OW(0); SB0(); OM(2, 2); SB0();
  OW(1); SB0(); OM(2, 3); SB0();
  OW(2); SB0(); OM(3, 2); SB0();
  OW(3); SB0(); OM(3, 3); SB0();
  OW(4); OW(5); SB0(); OW(6); OW(7);
  WAITL0();
  STAMP(3);
  CBAR();
  STAMP(4);
#define WR(u, i) wf[(u) & 1][i] = tr_frag(wp, (i) * 2048, (i) * 2048 + 512)
#define PM(u, i) acc[((u) >> 1) & 1] = MFMA(wf[(u) & 1][i], of[8 * ((u) & 1) + (i)], (((u) & 1) == 0 && (i) == 0) ? zero16 : acc[((u) >> 1) & 1])
#define PST(c, a) __builtin_amdgcn_raw_buffer_store_b128(__builtin_bit_cast(u32x4, pack8(acc[(c) & 1], a)), prs, pofs + (c) * 512 + (a) * 256, 0, PAUX)
  const unsigned fofs = lane < 4 ? WS_F + ((b * 16 + hp) * 4 + wid) * 256 + lane * 4 : OOB;
#define FLAG(n) __builtin_amdgcn_raw_buffer_store_b32((an << 4) | (n), prs, fofs, 0, 16)
  LDSP char* wp = smem + wa[0] + PBLK(0) * 32768;
  bf16x8 pk[2];
#pragma unroll
  for (int i = 0; i < 8; ++i) WR(0, i);
  SB0();
#pragma unroll
  for (int u = 0; u < 16; ++u) {
    if ((u & 1) && u < (P7 ? 15 : 13)) { WAITL0(); CBAR(); }
    if (u < 15) { int wo_ = wa[(u + 1) & 1] + PBLK((u + 1) >> 1) * 32768; asm volatile("" : "+v"(wo_)); wp = smem + wo_; }
    const int c = (u >> 1) - 1;
#pragma unroll
    for (int i = 0; i < 8; ++i) {
      PM(u, i); SB0();
      if (u == 0) { if (i < 4) { of[8 + 2 * i] = *(LDSP bf16x8*)(smem + (ooff ^ 8192) + 2 * i * 1024); of[9 + 2 * i] = *(LDSP bf16x8*)(smem + (ooff ^ 8192) + (2 * i + 1) * 1024); } }
      if (u < 15) { if (i < 2) { WR(u + 1, 2 * i); WR(u + 1, 2 * i + 1); } else if (i < 6) WR(u + 1, i + 2); }
      if ((u & 1) == 0 && u > 0) {
        if (i == 4) PST(c, 0);
        if (i == 5) PST(c, 1);
      }
      SB0();
    }
    if ((u & 3) == 3) STAMP(5 + (u >> 2));
  }
  PST(7, 0); PST(7, 1);
  WAITV(0);
#if !(PAUX & 16)
  __builtin_amdgcn_fence(__ATOMIC_RELEASE, "agent");
  WAITV(0);
#endif
  FLAG(8);
  STAMP(9);
  {
    const unsigned vofs = lane < 4 ? WS_F + ((b * 16 + 4 * wid + lane) * 4 + (hp >> 2)) * 256 + (hp & 3) * 4 : OOB;
#pragma unroll 1
    for (int spin = 0; spin < SPIN_CAP; ++spin) {
      const unsigned f = ld_sc1(prs, vofs);
      const bool ok = __all(lane >= 4 || (f >> 4) == nonce);
      const unsigned a2 = ld_sc1(prs, aofs);
      if (__any(a2 != an)) { an = a2; FLAG(8); }
      if (ok) break;
      __builtin_amdgcn_s_sleep(8);
    }
  }
}

extern "C" void kernel_launch(void* const* d_in, const int* in_sizes, int n_in, void* d_out, int out_size, void* d_ws,
                              size_t ws_size, hipStream_t stream) {
  fused_kernel<<<256, 768, 0, stream>>>((const char*)d_in[0], (const char*)d_in[1], (const char*)d_in[2],
                                        (const char*)d_in[3], (const char*)d_in[4], (char*)d_ws, (char*)d_out);
}
```

```cpp
#include <hip/hip_runtime.h>
#include <hip/hip_bf16.h>

typedef __bf16 bf16x4 __attribute__((ext_vector_type(4)));
typedef __bf16 bf16x8 __attribute__((ext_vector_type(8)));
typedef float f32x16 __attribute__((ext_vector_type(16)));
typedef float f32x4 __attribute__((ext_vector_type(4)));
typedef float f32x2 __attribute__((ext_vector_type(2)));
typedef unsigned u32x4 __attribute__((ext_vector_type(4)));
typedef unsigned u32x2 __attribute__((ext_vector_type(2)));
#define LDSP __attribute__((address_space(3)))
#define DI __device__ __forceinline__
#define MFMA(a, b, c) __builtin_amdgcn_mfma_f32_32x32x16_bf16((a), (b), (c), 0, 0, 0)
#define BAR() do { asm volatile("" ::: "memory"); __builtin_amdgcn_s_barrier(); asm volatile("" ::: "memory"); } while (0)
#define WAITV(n) asm volatile("s_waitcnt vmcnt(%0)" :: "i"(n) : "memory")
#define SB0() __builtin_amdgcn_sched_barrier(0)
#define WAITL0() do { asm volatile("" ::: "memory"); SB0(); __builtin_amdgcn_s_waitcnt(0xC07F); SB0(); asm volatile("" ::: "memory"); } while (0)
#define DMA(rs, vo, so, l, aux) __builtin_amdgcn_raw_ptr_buffer_load_lds(rs, (LDSP unsigned*)(l), 16, vo, so, 0, aux)
#define DMA4(rs, vo, so, l) __builtin_amdgcn_raw_ptr_buffer_load_lds(rs, (LDSP unsigned*)(l), 4, vo, so, 0, 0)
#define RSRC(p, n) __builtin_amdgcn_make_buffer_rsrc((void*)(p), 0, (n), 0x00020000)
constexpr int SC1 = 16, NT = 2;

#define PDELAY 16
#define OAUX 16
#define LPRIO 0
#define CPRIO 0
#define KVAUX 2
#define WPOS 2
#define PSLEEP 4
#define VSLEEP 40
#define CODE_BYTES 14592

constexpr int LDS_X = 0;
constexpr int LDS_K = 32768;
constexpr int LDS_W = 65536;
constexpr int LDS_V = 131072;
constexpr int LDS_TOTAL = 163840;
constexpr int LDS_TAG = LDS_X + 24576;
constexpr int LDS_CNT = LDS_X + 8192;
#define PBLK(c) ((0x41324132u >> (4 * (c))) & 15)
constexpr int WS_F = 16 << 20;
constexpr int WS_A = WS_F + (256 << 10);
constexpr int WS_BYTES = WS_A + 4096;
constexpr unsigned OOB = 0x7ffffff0u;
constexpr int SPIN_CAP = 1 << 16;

DI bf16x8 tr_frag(LDSP char* p, int off_lo, int off_hi) {
  bf16x4 a = __builtin_amdgcn_ds_read_tr16_b64_v4bf16((LDSP bf16x4*)(p + off_lo));
  bf16x4 b = __builtin_amdgcn_ds_read_tr16_b64_v4bf16((LDSP bf16x4*)(p + off_hi));
  return __builtin_shufflevector(a, b, 0, 1, 2, 3, 4, 5, 6, 7);
}
DI unsigned ld_sc1(__amdgpu_buffer_rsrc_t rs, unsigned ofs) {
  unsigned v;
  asm volatile("buffer_load_dword %0, %1, %2, 0 offen sc1\n\ts_waitcnt vmcnt(0)" : "=&v"(v) : "v"(ofs), "s"(rs) : "memory");
  return v;
}
DI bf16x8 pack8(const f32x16& x, int s) {
  bf16x8 p;
#pragma unroll
  for (int j = 0; j < 8; ++j) p[j] = (__bf16)x[8 * s + j];
  return p;
}
struct FragSet { bf16x8 a[2][4]; bf16x8 b[2]; };

#define STAMP(k)

__global__ __launch_bounds__(512, 2) void fused_kernel(const char* __restrict__ x, const char* __restrict__ kc,
                                                       const char* __restrict__ vc, const char* __restrict__ wq,
                                                       const char* __restrict__ wo, char* part, char* out) {
  __shared__ __attribute__((aligned(16))) char smem_[LDS_TOTAL];
  LDSP char* smem = (LDSP char*)smem_;
  const unsigned long long pc0 = __builtin_amdgcn_s_getpc() & ~255ull;
  const int wid0 = __builtin_amdgcn_readfirstlane(threadIdx.x >> 6);
  const int wid = wid0 < 4 ? wid0 : wid0 - 8;
  const int lane = threadIdx.x & 63;
  const int bid = blockIdx.x;
  const int hp = (bid & 7) * 2 + ((bid >> 3) & 1);
  const int b = bid >> 4;
  const int grpB = b & 1;
  const int u0 = b >> 1;
  const int swp = b & 1;

  if (__builtin_expect(wid == 1, 1)) {
    DMA4(RSRC(pc0, CODE_BYTES), lane < 8 ? (unsigned)(((bid >> 3) * 8 + lane) * 64) : OOB, 0, smem + LDS_X + 28672);
  }
  asm volatile("" ::: "memory");
  const int lw = wid + 4;
  const int hi = lane >> 5;
  const auto rq = RSRC(wq, 4 << 20);
  const auto rx = RSRC(x, 1 << 20);
  const int wq_e = (8 * lw + hi) * 8192 + hp * 512 + (((lane & 31) ^ (hi << 2)) << 4);
  const int wq_o = (8 * lw + 2 + hi) * 8192 + hp * 512 + (((lane & 31) ^ ((2 + hi) << 2)) << 4);
  const int xg = b * 65536 + (8 * lw + (lane >> 3)) * 1024 + (((lane & 7) ^ (4 * (lw & 1) + (lane >> 4))) << 4);
  auto issue_pair = [&](int j, int blkofs) {
    const int ku = (u0 + j) & 7;
    LDSP char* d = smem + blkofs + lw * 4096;
#pragma unroll
    for (int sg = 0; sg < 2; ++sg) {
      const int go = (2 * ku + (sg ^ swp)) * 32 * 8192;
      DMA(rq, wq_e, go, d + sg * 16384, 0);
      DMA(rq, wq_o, go, d + sg * 16384 + 1024, 0);
      DMA(rq, wq_e, go + 4 * 8192, d + sg * 16384 + 2048, 0);
      DMA(rq, wq_o, go + 4 * 8192, d + sg * 16384 + 3072, 0);
      if (sg == 0) {
        DMA(rx, xg, ku * 128, smem + LDS_X + (j & 3) * 8192 + lw * 1024, 0);
        DMA(rx, xg, 32768 + ku * 128, smem + LDS_X + (j & 3) * 8192 + 4096 + lw * 1024, 0);
      }
    }
  };
  if (__builtin_expect(wid < 0, 1)) issue_pair(0, LDS_W);
  asm volatile("" ::: "memory");

  if (wid < 0) {
    __builtin_amdgcn_s_setprio(LPRIO);
    const auto ro = RSRC(wo, 4 << 20);
    const auto rk = RSRC(kc, 8 << 20);
    const auto rv = RSRC(vc, 8 << 20);
    const int c16 = ((lane & 15) ^ ((lane >> 4) << 2)) << 4;
    const int wog = (hp * 256 + 64 * lw + (lane >> 3)) * 1024 + (((lane & 7) ^ (((lane >> 4) & 1) << 2)) << 4);
    const int kvb = (b * 32 + hp * 2) * 16384;
    const int vg = kvb + (32 * lw + (lane >> 4)) * 256 + c16;
    const int kg = kvb + (4 * lw + (lane >> 4)) * 256 + (((lane & 15) ^ (4 * (lw & 3) + (lane >> 4))) << 4);
    auto issue_k = [&]() {
#pragma unroll
      for (int i = 0; i < 8; ++i) DMA(rk, kg, i * 4096, smem + LDS_K + i * 4096 + lw * 1024, KVAUX);
    };
    auto issue_v = [&]() {
#pragma unroll
      for (int i = 0; i < 8; ++i) DMA(rv, vg, i * 1024, smem + LDS_V + lw * 8192 + i * 1024, KVAUX);
    };
    auto issue_wo = [&](int c) {
      LDSP char* d = smem + PBLK(c) * 32768 + lw * 8192;
#pragma unroll
      for (int i = 0; i < 8; ++i) DMA(ro, wog, c * 128 + i * 8192, d + i * 1024, 0);
    };
    int pfo = grpB ? (hp * 256 + (b >> 1) * 32 + lw * 8) * 1024 + lane * 64
                   : b * 65536 + ((hp & 1) * 32 + lw * 8) * 1024 + lane * 64;
    asm volatile("" : "+v"(pfo));
    if (!grpB) {
      DMA4(rx, pfo, 65536, smem + LDS_K + lw * 1024);
      DMA4(rx, pfo, 65536 + 4096, smem + LDS_K + lw * 1024);
      DMA4(rx, pfo, 0, smem + LDS_K + lw * 1024);
      DMA4(rx, pfo, 4096, smem + LDS_K + lw * 1024);
      issue_k();
      { const int pw = (hp * 256 + (b >> 1) * 32 + lw * 8) * 1024 + lane * 64;
        DMA4(ro, pw, 0, smem + LDS_W + 32768 + lw * 4096); DMA4(ro, pw, 4096, smem + LDS_W + 32768 + lw * 4096); }
    }
    issue_pair(1, LDS_W + 32768);
    WAITV(10); BAR();
    unsigned tag;
    asm volatile("ds_read_b32 %0, %1\n\ts_waitcnt lgkmcnt(0)" : "=v"(tag) : "v"(LDS_TAG) : "memory");
    tag = __builtin_amdgcn_readfirstlane(tag);
    {
      int bo = LDS_W + 65536;
#pragma unroll 1
      for (int j = 2; j < 8; ++j) {
        issue_pair(j, bo);
        bo = (bo == LDS_W + 65536) ? LDS_W : bo + 32768;
        WAITV(10); BAR();
      }
    }
    if (grpB) {
      issue_k();
      WAITV(8); BAR();
      issue_v();
      WAITV(8); BAR();
      issue_wo(0);
      WAITV(8); BAR();
      issue_wo(1);
    } else {
      issue_v();
      WAITV(8); BAR();
      BAR();
      issue_wo(0); issue_wo(1);
      WAITV(16); BAR();
    }
    issue_wo(2);
    WAITV(16); BAR();
    issue_wo(3);
    WAITV(16); BAR();
    if (lw == 3) *(volatile LDSP int*)(smem + LDS_CNT) = 0;
#pragma unroll 1
    for (int c = 4; c < 8; ++c) {
      issue_wo(c);
      WAITV(16); BAR();
    }
    WAITV(8); BAR();
    WAITV(0); WAITL0(); BAR();

    const auto prs = RSRC(part, WS_BYTES);
    const auto rout = RSRC(out, 1 << 20);
    const int fofs = WS_F + ((b * 16 + 4 * lw + (lane & 3)) * 4 + (hp >> 2)) * 256 + (hp & 3) * 4;
    const int inb = ((b * 16 + hp) * 16 + 4 * lw) * 4096;
    u32x4 v[4][4];
    unsigned got = 0;
    __builtin_amdgcn_s_sleep(PDELAY);
#pragma unroll 1
    for (int spin = 0; spin < SPIN_CAP; ++spin) {
      const unsigned f = ld_sc1(prs, (lane < 4 && !((got >> lane) & 1)) ? fofs : OOB);
      const unsigned rdy = (unsigned)__ballot(lane < 4 && f == tag) | (spin == SPIN_CAP - 1 ? 15u : 0u);
      const unsigned nw = rdy & ~got;
#pragma unroll
      for (int p = 0; p < 4; ++p)
        if (nw & (1 << p)) {
#pragma unroll
          for (int i = 0; i < 4; ++i) v[p][i] = __builtin_amdgcn_raw_buffer_load_b128(prs, lane * 16 + i * 1024, inb + p * 4096, SC1);
        }
      got |= rdy;
      if (got == 15) break;
      __builtin_amdgcn_s_sleep(PSLEEP);
    }
    LDSP f32x4* slab = (LDSP f32x4*)(smem + LDS_W + lw * 8192) + lane;
#pragma unroll
    for (int i = 0; i < 4; ++i)
#pragma unroll
      for (int e = 0; e < 2; ++e) {
        f32x4 t;
#pragma unroll
        for (int z = 0; z < 2; ++z) {
          t[2 * z] = (__uint_as_float(v[0][i][2 * e + z] << 16) + __uint_as_float(v[1][i][2 * e + z] << 16)) + (__uint_as_float(v[2][i][2 * e + z] << 16) + __uint_as_float(v[3][i][2 * e + z] << 16));
          t[2 * z + 1] = (__uint_as_float(v[0][i][2 * e + z] & 0xffff0000u) + __uint_as_float(v[1][i][2 * e + z] & 0xffff0000u)) + (__uint_as_float(v[2][i][2 * e + z] & 0xffff0000u) + __uint_as_float(v[3][i][2 * e + z] & 0xffff0000u));
        }
        slab[(2 * i + e) * 64] = t;
      }
    WAITL0();
    if (lane == 0) __hip_atomic_fetch_add((LDSP int*)(smem + LDS_CNT), 1, __ATOMIC_RELAXED, __HIP_MEMORY_SCOPE_WORKGROUP);
#pragma unroll 1
    for (int spin = 0; spin < (SPIN_CAP << 8); ++spin) {
      if (__builtin_amdgcn_readfirstlane(*(volatile LDSP int*)(smem + LDS_CNT)) >= 4) break;
      __builtin_amdgcn_s_sleep(1);
    }
    asm volatile("" ::: "memory");
    LDSP f32x4* src = (LDSP f32x4*)(smem + LDS_W) + lw * 128 + lane;
    const f32x4 s0 = (src[0] + src[512]) + (src[1024] + src[1536]);
    const f32x4 s1 = (src[64] + src[576]) + (src[1088] + src[1600]);
    bf16x8 o;
#pragma unroll
    for (int e = 0; e < 4; ++e) { o[e] = (__bf16)s0[e]; o[4 + e] = (__bf16)s1[e]; }
    const int c = 2 * lw + (lane >> 5);
    const int row = b * 64 + 32 * ((hp >> 2) & 1) + 8 * (hp & 3) + (lane & 7);
    const int col = 64 * c + 32 * (hp >> 3) + 16 * ((lane >> 4) & 1) + 8 * ((lane >> 3) & 1);
    __builtin_amdgcn_raw_buffer_store_b128(__builtin_bit_cast(u32x4, o), rout, row * 1024 + col * 2, 0, OAUX);
    return;
  }

  __builtin_amdgcn_s_setprio(CPRIO);
  const int h = wid >> 1, qh = wid & 1;
  const int r = lane & 31, hh = lane >> 5;
  const int blk = (lane >> 4) & 1, q4 = (lane >> 2) & 3, p4 = lane & 3;
  const int pp = ((p4 & 1) << 1) | (p4 >> 1);
  const int pir = (r & 19) | ((r & 4) << 1) | ((r & 8) >> 1);
  STAMP(0);
  const auto prs = RSRC(part, WS_BYTES);
  if (wid == 0) {
    const unsigned f = ld_sc1(prs, WS_F + ((b * 16 + (lane & 15)) * 4 + (hp >> 2)) * 256 + (hp & 3) * 4);
    unsigned cand = 1;
#pragma unroll 1
    while (__any(f == cand)) ++cand;
    __builtin_amdgcn_raw_buffer_store_b32(cand, prs, lane == 0 ? WS_A + b * 256 + hp * 4 : OOB, 0, SC1);
    *(LDSP unsigned*)(smem + LDS_TAG) = cand;
  }
  WAITL0();

  int woff[4], xoff[4], voff[4], koff[8], wa[2];
  const int xrow = 32 * qh + r;
#pragma unroll
  for (int i = 0; i < 4; ++i) {
    woff[i] = LDS_W + (8 * hh + q4) * 512 + h * 256 + ((i ^ q4) << 6) + blk * 32 + pp * 8;
    xoff[i] = LDS_X + xrow * 128 + (((2 * (i ^ (2 * swp)) + hh) ^ ((xrow >> 1) & 7)) << 4);
    voff[i] = LDS_V + h * 16384 + (8 * hh + q4) * 256 + ((i ^ q4) << 6) + blk * 32 + pp * 8;
  }
#pragma unroll
  for (int s = 0; s < 8; ++s) koff[s] = LDS_K + h * 16384 + pir * 256 + (((2 * s + hh) ^ (pir & 15)) << 4);
#pragma unroll
  for (int s = 0; s < 2; ++s) wa[s] = (s ^ h) * 16384 + (8 * hh + q4) * 128 + ((h ^ (q4 >> 1)) << 6) + blk * 32 + pp * 8;
  int ooff = LDS_X + (qh * 16 + h * 8) * 1024 + lane * 16;
  int pofs = ((b * 16 + 4 * wid + (r >> 3)) * 16 + hp) * 4096 + hh * 128 + (r & 7) * 16;
  asm volatile("" : "+v"(voff[0]), "+v"(voff[1]), "+v"(voff[2]), "+v"(voff[3]), "+v"(ooff), "+v"(wa[0]), "+v"(wa[1]), "+v"(pofs));
  asm volatile("" : "+v"(koff[0]), "+v"(koff[1]), "+v"(koff[2]), "+v"(koff[3]), "+v"(koff[4]), "+v"(koff[5]), "+v"(koff[6]), "+v"(koff[7]));
  const f32x16 zero16 = {0.f, 0.f, 0.f, 0.f, 0.f, 0.f, 0.f, 0.f, 0.f, 0.f, 0.f, 0.f, 0.f, 0.f, 0.f, 0.f};
  f32x16 accq[4] = {zero16, zero16, zero16, zero16};

#define MM(c, i) accq[(i) & 3] = MFMA(c.a[(i) >> 2][(i) & 3], c.b[(i) >> 2], accq[(i) & 3])
  auto rd = [&](FragSet& n, LDSP char* wb, LDSP char* xb, int par, int g) {
    if (g == 0) { n.b[0] = *(LDSP bf16x8*)(xb + xoff[2 * par]); n.a[0][0] = tr_frag(wb + woff[0], 0, 2048); }
    if (g == 1) { n.a[0][1] = tr_frag(wb + woff[1], 0, 2048); n.a[0][2] = tr_frag(wb + woff[2], 0, 2048); }
    if (g == 2) { n.a[0][3] = tr_frag(wb + woff[3], 0, 2048); n.b[1] = *(LDSP bf16x8*)(xb + xoff[2 * par + 1]); }
    if (g == 3) { n.a[1][0] = tr_frag(wb + woff[0], 8192, 10240); n.a[1][1] = tr_frag(wb + woff[1], 8192, 10240); }
    if (g == 4) { n.a[1][2] = tr_frag(wb + woff[2], 8192, 10240); n.a[1][3] = tr_frag(wb + woff[3], 8192, 10240); }
  };
  FragSet f0, f1;
  asm volatile("" : "+v"(accq[0]), "+v"(accq[1]), "+v"(accq[2]), "+v"(accq[3]));
  BAR();
  STAMP(1);
#pragma unroll
  for (int g = 0; g < 5; ++g) rd(f0, smem, smem, 0, g);
  const unsigned tag = __builtin_amdgcn_readfirstlane(*(LDSP unsigned*)(smem + LDS_TAG));
  SB0();
#pragma unroll
  for (int i = 0; i < 8; ++i) { MM(f0, i); SB0(); rd(f1, smem + 16384, smem, 1, i); SB0(); }
  MM(f1, 0); SB0(); MM(f1, 1); SB0();
  WAITL0();
  {
    int sl = 0;
#pragma unroll 1
    for (int j = 1; j < 8; ++j) {
      BAR();
      sl = (sl == 4) ? 0 : sl + 2;
      LDSP char* wb = smem + sl * 16384;
      LDSP char* xb = smem + (j & 3) * 8192;
      rd(f0, wb, xb, 0, 0); SB0();
#pragma unroll
      for (int i = 2; i < 8; ++i) { MM(f1, i); SB0(); rd(f0, wb, xb, 0, i - 1); SB0(); }
#pragma unroll
      for (int i = 0; i < 8; ++i) { MM(f0, i); SB0(); rd(f1, wb + 16384, xb, 1, i); SB0(); }
      MM(f1, 0); SB0(); MM(f1, 1); SB0();
      WAITL0();
    }
  }
  WAITV(0);
  BAR();
  STAMP(12);

  bf16x8 kf[2][8], qf[8], vf[4][4], pf[4];
  f32x16 accs[2], acco[4];
#define QM(dt, ks) accq[dt] = MFMA(f1.a[ks][dt], f1.b[ks], accq[dt])
#define QP(i) qf[i] = pack8(accq[(i) >> 1], (i) & 1)
#define KR(kt, s) kf[kt][s] = *(LDSP bf16x8*)(smem + koff[s] + (kt) * 8192)
#define SM(kt, s) accs[kt] = MFMA(kf[kt][s], qf[s], (s) == 0 ? zero16 : accs[kt])
#define VR(dt, s) vf[dt][s] = tr_frag(smem + voff[dt], (s) * 4096, (s) * 4096 + 1024)
  const unsigned aofs = lane < 4 ? WS_A + b * 256 + (4 * wid + lane) * 4 : OOB;
  unsigned an = __builtin_amdgcn_raw_buffer_load_b32(prs, aofs, 0, SC1);
  KR(0, 0); KR(0, 1); SB0();
  QM(0, 1); SB0(); KR(0, 2); KR(0, 3); SB0();
  QM(1, 1); SB0(); KR(0, 4); KR(0, 5); SB0();
  QM(2, 0); SB0(); KR(0, 6); KR(0, 7); SB0();
  QM(2, 1); SB0(); QP(0); SB0();
  QM(3, 0); SB0(); QP(1); SB0();
  QM(3, 1); SB0(); QP(2); SB0();
  SM(0, 0); SB0(); QP(3); KR(1, 0); SB0();
  SM(0, 1); SB0(); QP(4); KR(1, 1); SB0();
  SM(0, 2); SB0(); QP(5); KR(1, 2); SB0();
  SM(0, 3); SB0(); QP(6); KR(1, 3); SB0();
  SM(0, 4); SB0(); QP(7); KR(1, 4); SB0();
  SM(0, 5); SB0(); KR(1, 5); KR(1, 6); KR(1, 7); SB0();
  SM(0, 6); SM(0, 7); SB0(); SM(1, 0); SM(1, 1); SM(1, 2); SM(1, 3); SB0();
  float mx = fmaxf(fmaxf(accs[0][0], accs[0][1]), accs[0][2]);
  mx = fmaxf(fmaxf(mx, accs[0][3]), accs[0][4]); mx = fmaxf(fmaxf(mx, accs[0][5]), accs[0][6]);
  asm volatile("" : "+v"(mx)); SB0(); SM(1, 4); SB0();
  mx = fmaxf(fmaxf(mx, accs[0][7]), accs[0][8]); mx = fmaxf(fmaxf(mx, accs[0][9]), accs[0][10]); mx = fmaxf(fmaxf(mx, accs[0][11]), accs[0][12]);
  asm volatile("" : "+v"(mx)); SB0(); SM(1, 5); SB0();
  mx = fmaxf(fmaxf(mx, accs[0][13]), accs[0][14]); mx = fmaxf(mx, accs[0][15]);
  asm volatile("" : "+v"(mx)); SB0(); SM(1, 6); SM(1, 7); SB0();
  {
    float m2 = fmaxf(fmaxf(accs[1][0], accs[1][1]), accs[1][2]);
#pragma unroll
    for (int i = 3; i < 9; i += 2) m2 = fmaxf(fmaxf(m2, accs[1][i]), accs[1][i + 1]);
#pragma unroll
    for (int i = 9; i < 15; i += 2) mx = fmaxf(fmaxf(mx, accs[1][i]), accs[1][i + 1]);
    mx = fmaxf(fmaxf(mx, m2), accs[1][15]);
    auto sw = __builtin_amdgcn_permlane32_swap(__float_as_uint(mx), __float_as_uint(mx), false, false);
    mx = fmaxf(__uint_as_float(sw[0]), __uint_as_float(sw[1]));
  }
  const float sc = 0.08838834764831845f * 1.4426950408889634f;
  const float mb = mx * sc;
  f32x2 sum2 = {0.f, 0.f}, sum3 = {0.f, 0.f};
#pragma unroll
  for (int kt = 0; kt < 2; ++kt)
#pragma unroll
    for (int i = 0; i < 16; i += 2) {
      f32x2 t = {accs[kt][i], accs[kt][i + 1]};
      t = t * sc - mb;
      t[0] = __builtin_amdgcn_exp2f(t[0]); t[1] = __builtin_amdgcn_exp2f(t[1]);
      accs[kt][i] = t[0]; accs[kt][i + 1] = t[1];
      if (i & 2) sum3 += t; else sum2 += t;
    }
  sum2 += sum3;
  float sum = sum2[0] + sum2[1];
  {
    auto sw = __builtin_amdgcn_permlane32_swap(__float_as_uint(sum), __float_as_uint(sum), false, false);
    sum = __uint_as_float(sw[0]) + __uint_as_float(sw[1]);
  }
  float inv = __builtin_amdgcn_rcpf(sum);
  asm volatile("" : "+v"(inv), "+v"(accs[0]), "+v"(accs[1]));
  WAITL0();
  BAR();
  bf16x8 of[16], wf[2][8];
  f32x16 acc[2];
#define PH(s, hf) do { _Pragma("unroll") for (int j = 4 * (hf); j < 4 * (hf) + 4; j += 2) { \
    f32x2 t = {accs[(s) >> 1][8 * ((s) & 1) + j], accs[(s) >> 1][8 * ((s) & 1) + j + 1]}; t *= inv; \
    pf[s][j] = (__bf16)t[0]; pf[s][j + 1] = (__bf16)t[1]; } } while (0)
#define OM(dt, s) acco[dt] = MFMA(vf[dt][s], pf[s], (s) == 0 ? zero16 : acco[dt])
#define OW(i) do { of[i] = pack8(acco[(i) >> 1], (i) & 1); *(LDSP bf16x8*)(smem + ooff + (i) * 1024) = of[i]; } while (0)
  VR(0, 0); VR(1, 0); VR(2, 0); VR(3, 0); VR(0, 1); VR(1, 1); SB0();
  PH(0, 0); PH(0, 1); PH(1, 0); PH(1, 1); PH(2, 0);
  asm volatile("" : "+v"(pf[0]), "+v"(pf[1]));
  SB0();
  OM(0, 0); SB0(); VR(2, 1); VR(3, 1); SB0();
  OM(1, 0); SB0(); VR(0, 2); VR(0, 3); SB0();
  OM(2, 0); SB0(); PH(2, 1); VR(1, 2); SB0();
  OM(3, 0); SB0(); PH(3, 0); VR(1, 3); SB0();
  OM(0, 1); SB0(); PH(3, 1); VR(2, 2); SB0();
  OM(1, 1); SB0(); VR(2, 3); VR(3, 2); SB0();
  OM(2, 1); SB0(); VR(3, 3); SB0(); OM(3, 1); SB0(); OM(0, 2); SB0(); OM(0, 3); SB0(); OM(1, 2); SB0(); OM(1, 3); SB0();
  OW(0); SB0(); OM(2, 2); SB0();
  OW(1); SB0(); OM(2, 3); SB0();
  OW(2); SB0(); OM(3, 2); SB0();
  OW(3); SB0(); OM(3, 3); SB0();
  OW(4); OW(5); SB0(); OW(6); OW(7);
  WAITL0();
  BAR();
  STAMP(4);

#define WR(u, i) wf[(u) & 1][i] = tr_frag(wp, (i) * 2048, (i) * 2048 + 512)
#define PM(u, i) acc[((u) >> 1) & 1] = MFMA(wf[(u) & 1][i], of[8 * ((u) & 1) + (i)], (((u) & 1) == 0 && (i) == 0) ? zero16 : acc[((u) >> 1) & 1])
#define PST(c, a) __builtin_amdgcn_raw_buffer_store_b128(__builtin_bit_cast(u32x4, pack8(acc[(c) & 1], a)), prs, pofs + (c) * 512 + (a) * 256, 0, SC1)
  LDSP char* wp = smem + wa[0] + PBLK(0) * 32768;
#pragma unroll
  for (int i = 0; i < 8; ++i) WR(0, i);
  SB0();
#pragma unroll
  for (int u = 0; u < 16; ++u) {
    if ((u & 1) && u < 15) { WAITL0(); BAR(); }
    if (u < 15) { int wo_ = wa[(u + 1) & 1] + PBLK((u + 1) >> 1) * 32768; asm volatile("" : "+v"(wo_)); wp = smem + wo_; }
    const int c = (u >> 1) - 1;
#pragma unroll
    for (int i = 0; i < 8; ++i) {
      PM(u, i); SB0();
      if (u == 0 && i < 4) {
        of[8 + 2 * i] = *(LDSP bf16x8*)(smem + (ooff ^ 8192) + 2 * i * 1024);
        of[9 + 2 * i] = *(LDSP bf16x8*)(smem + (ooff ^ 8192) + (2 * i + 1) * 1024);
      }
      if (u < 15) { if (i < 2) { WR(u + 1, 2 * i); WR(u + 1, 2 * i + 1); } else if (i < 6) WR(u + 1, i + 2); }
      if ((u & 1) == 0 && u > 0) {
        if (i == 4) PST(c, 0);
        if (i == 5) PST(c, 1);
      }
      SB0();
    }
  }
  STAMP(8);
  PST(7, 0); PST(7, 1);
  const unsigned fofs = lane < 4 ? WS_F + ((b * 16 + hp) * 4 + wid) * 256 + lane * 4 : OOB;
  WAITV(0);
  __builtin_amdgcn_raw_buffer_store_b32(an, prs, fofs, 0, SC1);
  STAMP(9);
  __builtin_amdgcn_s_sleep(VSLEEP);
  const unsigned vofs = lane < 4 ? WS_F + ((b * 16 + 4 * wid + lane) * 4 + (hp >> 2)) * 256 + (hp & 3) * 4 : OOB;
#pragma unroll 1
  for (int spin = 0; spin < SPIN_CAP; ++spin) {
    const unsigned f = ld_sc1(prs, vofs);
    const bool ok = __all(lane >= 4 || f == tag);
    const unsigned a2 = ld_sc1(prs, aofs);
    if (__any(a2 != an)) { an = a2; __builtin_amdgcn_raw_buffer_store_b32(an, prs, fofs, 0, SC1); }
    if (ok) break;
    __builtin_amdgcn_s_sleep(8);
  }
}

extern "C" void kernel_launch(void* const* d_in, const int* in_sizes, int n_in, void* d_out, int out_size, void* d_ws,
                              size_t ws_size, hipStream_t stream) {
  fused_kernel<<<256, 512, 0, stream>>>((const char*)d_in[0], (const char*)d_in[1], (const char*)d_in[2],
                                        (const char*)d_in[3], (const char*)d_in[4], (char*)d_ws, (char*)d_out);
}
```

```cpp
#include <hip/hip_runtime.h>
#include <hip/hip_bf16.h>

typedef __bf16 bf16x4 __attribute__((ext_vector_type(4)));
typedef __bf16 bf16x8 __attribute__((ext_vector_type(8)));
typedef float f32x16 __attribute__((ext_vector_type(16)));
typedef float f32x4 __attribute__((ext_vector_type(4)));
typedef float f32x2 __attribute__((ext_vector_type(2)));
typedef unsigned u32x4 __attribute__((ext_vector_type(4)));
#define LDSP __attribute__((address_space(3)))
#define DI __device__ __forceinline__
#define MFMA(a, b, c) __builtin_amdgcn_mfma_f32_32x32x16_bf16((a), (b), (c), 0, 0, 0)
#define BAR() do { asm volatile("" ::: "memory"); __builtin_amdgcn_s_barrier(); asm volatile("" ::: "memory"); } while (0)
#define WAITV(n) asm volatile("s_waitcnt vmcnt(%0)" :: "i"(n) : "memory")
#define SB0() __builtin_amdgcn_sched_barrier(0)
#define WAITL0() do { asm volatile("" ::: "memory"); SB0(); __builtin_amdgcn_s_waitcnt(0xC07F); SB0(); asm volatile("" ::: "memory"); } while (0)
#define DMA(rs, vo, so, l, aux) __builtin_amdgcn_raw_ptr_buffer_load_lds(rs, (LDSP unsigned*)(l), 16, vo, so, 0, aux)
#define DMA4(rs, vo, so, l) __builtin_amdgcn_raw_ptr_buffer_load_lds(rs, (LDSP unsigned*)(l), 4, vo, so, 0, 0)
#define RSRC(p, n) __builtin_amdgcn_make_buffer_rsrc((void*)(p), 0, (n), 0x00020000)
constexpr int SC1 = 16, NT = 2;
constexpr int CODE_BYTES = 14848;

constexpr int LDS_X = 0;
constexpr int LDS_K = 32768;
constexpr int LDS_W = 65536;
constexpr int LDS_V = 131072;
constexpr int LDS_TOTAL = 163840;
constexpr int LDS_TAG = LDS_X + 24576;
constexpr int LDS_DUMP = LDS_X + 28672;
constexpr int LDS_CNT = LDS_X + 8192;
#define PBLK(c) ((0x41324132u >> (4 * (c))) & 15)
constexpr int WS_F = 16 << 20;
constexpr int WS_A = WS_F + (256 << 10);
constexpr int WS_BYTES = WS_A + 4096;
constexpr unsigned OOB = 0x7ffffff0u;
constexpr int SPIN_CAP = 1 << 16;

DI bf16x8 tr_frag(LDSP char* p, int off_lo, int off_hi) {
  bf16x4 a = __builtin_amdgcn_ds_read_tr16_b64_v4bf16((LDSP bf16x4*)(p + off_lo));
  bf16x4 b = __builtin_amdgcn_ds_read_tr16_b64_v4bf16((LDSP bf16x4*)(p + off_hi));
  return __builtin_shufflevector(a, b, 0, 1, 2, 3, 4, 5, 6, 7);
}
DI unsigned ld_sc1(__amdgpu_buffer_rsrc_t rs, unsigned ofs) {
  unsigned v;
  asm volatile("buffer_load_dword %0, %1, %2, 0 offen sc1\n\ts_waitcnt vmcnt(0)" : "=&v"(v) : "v"(ofs), "s"(rs) : "memory");
  return v;
}
DI bf16x8 pack8(const f32x16& x, int s) {
  bf16x8 p;
#pragma unroll
  for (int j = 0; j < 8; ++j) p[j] = (__bf16)x[8 * s + j];
  return p;
}
struct FragSet { bf16x8 a[2][4]; bf16x8 b[2]; };

__global__ __launch_bounds__(512, 2) void fused_kernel(const char* __restrict__ x, const char* __restrict__ kc,
                                                       const char* __restrict__ vc, const char* __restrict__ wq,
                                                       const char* __restrict__ wo, char* part, char* out) {
  __shared__ __attribute__((aligned(16))) char smem_[LDS_TOTAL];
  LDSP char* smem = (LDSP char*)smem_;
  const unsigned long long pc0 = __builtin_amdgcn_s_getpc() & ~255ull;
  const int wid0 = __builtin_amdgcn_readfirstlane(threadIdx.x >> 6);
  const int wid = wid0 < 4 ? wid0 : wid0 - 8;
  const int lane = threadIdx.x & 63;
  const int bid = blockIdx.x;
  const int hp = (bid & 7) * 2 + ((bid >> 3) & 1);
  const int b = bid >> 4;
  const int grpB = b & 1;
  const int u0 = b >> 1;
  const int rot = b >> 1;
  const int swp = b & 1;

  if (__builtin_expect(wid == 1, 1)) {
    DMA4(RSRC(pc0, CODE_BYTES), lane < 8 ? (unsigned)(((bid >> 3) * 8 + lane) * 64) : OOB, 0, smem + LDS_DUMP);
  }
  asm volatile("" ::: "memory");
  const int lw = wid + 4;
  const int hi = lane >> 5;
  const auto rq = RSRC(wq, 4 << 20);
  const auto rx = RSRC(x, 1 << 20);
  const int wq_e = (8 * lw + hi) * 8192 + hp * 512 + (((lane & 31) ^ (hi << 2)) << 4);
  const int wq_o = (8 * lw + 2 + hi) * 8192 + hp * 512 + (((lane & 31) ^ ((2 + hi) << 2)) << 4);
  const int xg = b * 65536 + (8 * lw + (lane >> 3)) * 1024 + (((lane & 7) ^ (4 * (lw & 1) + (lane >> 4))) << 4);
  auto issue_pair = [&](int j, int blkofs) {
    const int ku = (u0 + j) & 7;
    LDSP char* d = smem + blkofs + lw * 4096;
#pragma unroll
    for (int sg = 0; sg < 2; ++sg) {
      const int go = (2 * ku + (sg ^ swp)) * 32 * 8192;
      DMA(rq, wq_e, go, d + sg * 16384, 0);
      DMA(rq, wq_o, go, d + sg * 16384 + 1024, 0);
      DMA(rq, wq_e, go + 4 * 8192, d + sg * 16384 + 2048, 0);
      DMA(rq, wq_o, go + 4 * 8192, d + sg * 16384 + 3072, 0);
      if (sg == 0) {
        DMA(rx, xg, ku * 128, smem + LDS_X + (j & 3) * 8192 + lw * 1024, 0);
        DMA(rx, xg, 32768 + ku * 128, smem + LDS_X + (j & 3) * 8192 + 4096 + lw * 1024, 0);
      }
    }
  };
  if (__builtin_expect(wid < 0, 1)) issue_pair(0, LDS_W);
  asm volatile("" ::: "memory");

  if (wid < 0) {
    const auto ro = RSRC(wo, 4 << 20);
    const auto rk = RSRC(kc, 8 << 20);
    const auto rv = RSRC(vc, 8 << 20);
    const int c16 = ((lane & 15) ^ ((lane >> 4) << 2)) << 4;
    const int wog = (hp * 256 + 64 * lw + (lane >> 3)) * 1024 + (((lane & 7) ^ (((lane >> 4) & 1) << 2)) << 4);
    const int kvb = (b * 32 + hp * 2) * 16384;
    const int vg = kvb + (32 * lw + (lane >> 4)) * 256 + c16;
    const int kg = kvb + (4 * lw + (lane >> 4)) * 256 + (((lane & 15) ^ (4 * (lw & 3) + (lane >> 4))) << 4);
    auto issue_k = [&]() {
#pragma unroll
      for (int i = 0; i < 8; ++i) DMA(rk, kg, i * 4096, smem + LDS_K + i * 4096 + lw * 1024, NT);
    };
    auto issue_v = [&]() {
#pragma unroll
      for (int i = 0; i < 8; ++i) DMA(rv, vg, i * 1024, smem + LDS_V + lw * 8192 + i * 1024, NT);
    };
    auto issue_wo = [&](int c) {
      LDSP char* d = smem + PBLK(c) * 32768 + lw * 8192;
#pragma unroll
      for (int i = 0; i < 8; ++i) DMA(ro, wog, ((c + rot) & 7) * 128 + i * 8192, d + i * 1024, 0);
    };
    if (!grpB) {
      const int pfo = b * 65536 + ((hp & 1) * 32 + lw * 8) * 1024 + lane * 64;
      LDSP char* dump = smem + LDS_DUMP + 256 + lw * 256;
      DMA4(rx, pfo, 65536, dump);
      DMA4(rx, pfo, 65536 + 4096, dump);
      DMA4(rx, pfo, 0, dump);
      DMA4(rx, pfo, 4096, dump);
      issue_k();
    }
    issue_pair(1, LDS_W + 32768);
    WAITV(10); BAR();
    unsigned tag;
    asm volatile("ds_read_b32 %0, %1\n\ts_waitcnt lgkmcnt(0)" : "=v"(tag) : "v"(LDS_TAG) : "memory");
    tag = __builtin_amdgcn_readfirstlane(tag);
    {
      int bo = LDS_W + 65536;
#pragma unroll 1
      for (int j = 2; j < 8; ++j) {
        issue_pair(j, bo);
        bo = (bo == LDS_W + 65536) ? LDS_W : bo + 32768;
        WAITV(10); BAR();
      }
    }
    if (grpB) {
      issue_k();
      WAITV(8); BAR();
      issue_v();
      WAITV(8); BAR();
      issue_wo(0);
      WAITV(8); BAR();
      issue_wo(1);
    } else {
      issue_v();
      WAITV(8); BAR();
      BAR();
      issue_wo(0); issue_wo(1);
      WAITV(16); BAR();
    }
    issue_wo(2);
    WAITV(16); BAR();
    issue_wo(3);
    WAITV(16); BAR();
    if (lw == 3) *(volatile LDSP int*)(smem + LDS_CNT) = 0;
#pragma unroll 1
    for (int c = 4; c < 8; ++c) {
      issue_wo(c);
      WAITV(16); BAR();
    }
    WAITV(8); BAR();
    WAITV(0); WAITL0(); BAR();

    const auto prs = RSRC(part, WS_BYTES);
    const auto rout = RSRC(out, 1 << 20);
    const int fofs = WS_F + ((b * 16 + 4 * lw + (lane & 3)) * 4 + (hp >> 2)) * 256 + (hp & 3) * 4;
    const int inb = ((b * 16 + hp) * 16 + 4 * lw) * 4096;
    u32x4 v[4][4];
    unsigned got = 0;
    __builtin_amdgcn_s_sleep(16);
#pragma unroll 1
    for (int spin = 0; spin < SPIN_CAP; ++spin) {
      const unsigned f = ld_sc1(prs, (lane < 4 && !((got >> lane) & 1)) ? fofs : OOB);
      const unsigned rdy = (unsigned)__ballot(lane < 4 && f == tag) | (spin == SPIN_CAP - 1 ? 15u : 0u);
      const unsigned nw = rdy & ~got;
#pragma unroll
      for (int p = 0; p < 4; ++p)
        if (nw & (1 << p)) {
#pragma unroll
          for (int i = 0; i < 4; ++i) v[p][i] = __builtin_amdgcn_raw_buffer_load_b128(prs, lane * 16 + i * 1024, inb + p * 4096, SC1);
        }
      got |= rdy;
      if (got == 15) break;
      __builtin_amdgcn_s_sleep(4);
    }
    LDSP f32x4* slab = (LDSP f32x4*)(smem + LDS_W + lw * 8192) + lane;
#pragma unroll
    for (int i = 0; i < 4; ++i)
#pragma unroll
      for (int e = 0; e < 2; ++e) {
        f32x4 t;
#pragma unroll
        for (int z = 0; z < 2; ++z) {
          t[2 * z] = (__uint_as_float(v[0][i][2 * e + z] << 16) + __uint_as_float(v[1][i][2 * e + z] << 16)) + (__uint_as_float(v[2][i][2 * e + z] << 16) + __uint_as_float(v[3][i][2 * e + z] << 16));
          t[2 * z + 1] = (__uint_as_float(v[0][i][2 * e + z] & 0xffff0000u) + __uint_as_float(v[1][i][2 * e + z] & 0xffff0000u)) + (__uint_as_float(v[2][i][2 * e + z] & 0xffff0000u) + __uint_as_float(v[3][i][2 * e + z] & 0xffff0000u));
        }
        slab[(2 * i + e) * 64] = t;
      }
    WAITL0();
    if (lane == 0) __hip_atomic_fetch_add((LDSP int*)(smem + LDS_CNT), 1, __ATOMIC_RELAXED, __HIP_MEMORY_SCOPE_WORKGROUP);
#pragma unroll 1
    for (int spin = 0; spin < (SPIN_CAP << 8); ++spin) {
      if (__builtin_amdgcn_readfirstlane(*(volatile LDSP int*)(smem + LDS_CNT)) >= 4) break;
      __builtin_amdgcn_s_sleep(1);
    }
    asm volatile("" ::: "memory");
    LDSP f32x4* src = (LDSP f32x4*)(smem + LDS_W) + lw * 128 + lane;
    const f32x4 s0 = (src[0] + src[512]) + (src[1024] + src[1536]);
    const f32x4 s1 = (src[64] + src[576]) + (src[1088] + src[1600]);
    bf16x8 o;
#pragma unroll
    for (int e = 0; e < 4; ++e) { o[e] = (__bf16)s0[e]; o[4 + e] = (__bf16)s1[e]; }
    const int c = (2 * lw + (lane >> 5) + rot) & 7;
    const int row = b * 64 + 32 * ((hp >> 2) & 1) + 8 * (hp & 3) + (lane & 7);
    const int col = 64 * c + 32 * (hp >> 3) + 16 * ((lane >> 4) & 1) + 8 * ((lane >> 3) & 1);
    __builtin_amdgcn_raw_buffer_store_b128(__builtin_bit_cast(u32x4, o), rout, row * 1024 + col * 2, 0, SC1);
    return;
  }

  const int h = wid >> 1, qh = wid & 1;
  const int r = lane & 31, hh = lane >> 5;
  const int blk = (lane >> 4) & 1, q4 = (lane >> 2) & 3, p4 = lane & 3;
  const int pp = ((p4 & 1) << 1) | (p4 >> 1);
  const int pir = (r & 19) | ((r & 4) << 1) | ((r & 8) >> 1);
  const auto prs = RSRC(part, WS_BYTES);
  if (wid == 0) {
    const unsigned f = ld_sc1(prs, WS_F + ((b * 16 + (lane & 15)) * 4 + (hp >> 2)) * 256 + (hp & 3) * 4);
    unsigned cand = 1;
#pragma unroll 1
    while (__any(f == cand)) ++cand;
    __builtin_amdgcn_raw_buffer_store_b32(cand, prs, lane == 0 ? WS_A + b * 256 + hp * 4 : OOB, 0, SC1);
    *(LDSP unsigned*)(smem + LDS_TAG) = cand;
  }
  WAITL0();

  int woff[4], xoff[4], voff[4], koff[8], wa[2];
  const int xrow = 32 * qh + r;
#pragma unroll
  for (int i = 0; i < 4; ++i) {
    woff[i] = LDS_W + (8 * hh + q4) * 512 + h * 256 + ((i ^ q4) << 6) + blk * 32 + pp * 8;
    xoff[i] = LDS_X + xrow * 128 + (((2 * (i ^ (2 * swp)) + hh) ^ ((xrow >> 1) & 7)) << 4);
    voff[i] = LDS_V + h * 16384 + (8 * hh + q4) * 256 + ((i ^ q4) << 6) + blk * 32 + pp * 8;
  }
#pragma unroll
  for (int s = 0; s < 8; ++s) koff[s] = LDS_K + h * 16384 + pir * 256 + (((2 * s + hh) ^ (pir & 15)) << 4);
#pragma unroll
  for (int s = 0; s < 2; ++s) wa[s] = (s ^ h) * 16384 + (8 * hh + q4) * 128 + ((h ^ (q4 >> 1)) << 6) + blk * 32 + pp * 8;
  int ooff = LDS_X + (qh * 16 + h * 8) * 1024 + lane * 16;
  int pofs = ((b * 16 + 4 * wid + (r >> 3)) * 16 + hp) * 4096 + hh * 128 + (r & 7) * 16;
  asm volatile("" : "+v"(voff[0]), "+v"(voff[1]), "+v"(voff[2]), "+v"(voff[3]), "+v"(ooff), "+v"(wa[0]), "+v"(wa[1]), "+v"(pofs));
  asm volatile("" : "+v"(koff[0]), "+v"(koff[1]), "+v"(koff[2]), "+v"(koff[3]), "+v"(koff[4]), "+v"(koff[5]), "+v"(koff[6]), "+v"(koff[7]));
  const f32x16 zero16 = {0.f, 0.f, 0.f, 0.f, 0.f, 0.f, 0.f, 0.f, 0.f, 0.f, 0.f, 0.f, 0.f, 0.f, 0.f, 0.f};
  f32x16 accq[4] = {zero16, zero16, zero16, zero16};

#define MM(c, i) accq[(i) & 3] = MFMA(c.a[(i) >> 2][(i) & 3], c.b[(i) >> 2], accq[(i) & 3])
  auto rd = [&](FragSet& n, LDSP char* wb, LDSP char* xb, int par, int g) {
    if (g == 0) { n.b[0] = *(LDSP bf16x8*)(xb + xoff[2 * par]); n.a[0][0] = tr_frag(wb + woff[0], 0, 2048); }
    if (g == 1) { n.a[0][1] = tr_frag(wb + woff[1], 0, 2048); n.a[0][2] = tr_frag(wb + woff[2], 0, 2048); }
    if (g == 2) { n.a[0][3] = tr_frag(wb + woff[3], 0, 2048); n.b[1] = *(LDSP bf16x8*)(xb + xoff[2 * par + 1]); }
    if (g == 3) { n.a[1][0] = tr_frag(wb + woff[0], 8192, 10240); n.a[1][1] = tr_frag(wb + woff[1], 8192, 10240); }
    if (g == 4) { n.a[1][2] = tr_frag(wb + woff[2], 8192, 10240); n.a[1][3] = tr_frag(wb + woff[3], 8192, 10240); }
  };
  FragSet f0, f1;
  asm volatile("" : "+v"(accq[0]), "+v"(accq[1]), "+v"(accq[2]), "+v"(accq[3]));
  BAR();
#pragma unroll
  for (int g = 0; g < 5; ++g) rd(f0, smem, smem, 0, g);
  const unsigned tag = __builtin_amdgcn_readfirstlane(*(LDSP unsigned*)(smem + LDS_TAG));
  SB0();
#pragma unroll
  for (int i = 0; i < 8; ++i) { MM(f0, i); SB0(); rd(f1, smem + 16384, smem, 1, i); SB0(); }
  MM(f1, 0); SB0(); MM(f1, 1); SB0();
  WAITV(0);
  WAITL0();
  {
    int sl = 0;
#pragma unroll 1
    for (int j = 1; j < 8; ++j) {
      BAR();
      sl = (sl == 4) ? 0 : sl + 2;
      LDSP char* wb = smem + sl * 16384;
      LDSP char* xb = smem + (j & 3) * 8192;
      rd(f0, wb, xb, 0, 0); SB0();
#pragma unroll
      for (int i = 2; i < 8; ++i) { MM(f1, i); SB0(); rd(f0, wb, xb, 0, i - 1); SB0(); }
#pragma unroll
      for (int i = 0; i < 8; ++i) { MM(f0, i); SB0(); rd(f1, wb + 16384, xb, 1, i); SB0(); }
      MM(f1, 0); SB0(); MM(f1, 1); SB0();
      WAITL0();
    }
  }
  BAR();

  bf16x8 kf[2][8], qf[8], vf[4][4], pf[4];
  f32x16 accs[2], acco[4];
#define QM(dt, ks) accq[dt] = MFMA(f1.a[ks][dt], f1.b[ks], accq[dt])
#define QP(i) qf[i] = pack8(accq[(i) >> 1], (i) & 1)
#define KR(kt, s) kf[kt][s] = *(LDSP bf16x8*)(smem + koff[s] + (kt) * 8192)
#define SM(kt, s) accs[kt] = MFMA(kf[kt][s], qf[s], (s) == 0 ? zero16 : accs[kt])
#define VR(dt, s) vf[dt][s] = tr_frag(smem + voff[dt], (s) * 4096, (s) * 4096 + 1024)
  const unsigned aofs = lane < 4 ? WS_A + b * 256 + (4 * wid + lane) * 4 : OOB;
  unsigned an = __builtin_amdgcn_raw_buffer_load_b32(prs, aofs, 0, SC1);
  KR(0, 0); KR(0, 1); SB0();
  QM(0, 1); SB0(); KR(0, 2); KR(0, 3); SB0();
  QM(1, 1); SB0(); KR(0, 4); KR(0, 5); SB0();
  QM(2, 0); SB0(); KR(0, 6); KR(0, 7); SB0();
  QM(2, 1); SB0(); QP(0); SB0();
  QM(3, 0); SB0(); QP(1); SB0();
  QM(3, 1); SB0(); QP(2); SB0();
  SM(0, 0); SB0(); QP(3); KR(1, 0); SB0();
  SM(0, 1); SB0(); QP(4); KR(1, 1); SB0();
  SM(0, 2); SB0(); QP(5); KR(1, 2); SB0();
  SM(0, 3); SB0(); QP(6); KR(1, 3); SB0();
  SM(0, 4); SB0(); QP(7); KR(1, 4); SB0();
  SM(0, 5); SB0(); KR(1, 5); KR(1, 6); KR(1, 7); SB0();
  SM(0, 6); SM(0, 7); SB0(); SM(1, 0); SM(1, 1); SM(1, 2); SM(1, 3); SB0();
  float mx = fmaxf(fmaxf(accs[0][0], accs[0][1]), accs[0][2]);
  mx = fmaxf(fmaxf(mx, accs[0][3]), accs[0][4]); mx = fmaxf(fmaxf(mx, accs[0][5]), accs[0][6]);
  asm volatile("" : "+v"(mx)); SB0(); SM(1, 4); SB0();
  mx = fmaxf(fmaxf(mx, accs[0][7]), accs[0][8]); mx = fmaxf(fmaxf(mx, accs[0][9]), accs[0][10]); mx = fmaxf(fmaxf(mx, accs[0][11]), accs[0][12]);
  asm volatile("" : "+v"(mx)); SB0(); SM(1, 5); SB0();
  mx = fmaxf(fmaxf(mx, accs[0][13]), accs[0][14]); mx = fmaxf(mx, accs[0][15]);
  asm volatile("" : "+v"(mx)); SB0(); SM(1, 6); SM(1, 7); SB0();
  {
    float m2 = fmaxf(fmaxf(accs[1][0], accs[1][1]), accs[1][2]);
#pragma unroll
    for (int i = 3; i < 9; i += 2) m2 = fmaxf(fmaxf(m2, accs[1][i]), accs[1][i + 1]);
#pragma unroll
    for (int i = 9; i < 15; i += 2) mx = fmaxf(fmaxf(mx, accs[1][i]), accs[1][i + 1]);
    mx = fmaxf(fmaxf(mx, m2), accs[1][15]);
    auto sw = __builtin_amdgcn_permlane32_swap(__float_as_uint(mx), __float_as_uint(mx), false, false);
    mx = fmaxf(__uint_as_float(sw[0]), __uint_as_float(sw[1]));
  }
  const float sc = 0.08838834764831845f * 1.4426950408889634f;
  f32x2 sum2 = {0.f, 0.f}, sum3 = {0.f, 0.f};
#pragma unroll
  for (int kt = 0; kt < 2; ++kt)
#pragma unroll
    for (int i = 0; i < 16; i += 2) {
      f32x2 t = {accs[kt][i], accs[kt][i + 1]};
      t = (t - mx) * sc;
      t[0] = __builtin_amdgcn_exp2f(t[0]); t[1] = __builtin_amdgcn_exp2f(t[1]);
      accs[kt][i] = t[0]; accs[kt][i + 1] = t[1];
      if (i & 2) sum3 += t; else sum2 += t;
    }
  sum2 += sum3;
  float sum = sum2[0] + sum2[1];
  {
    auto sw = __builtin_amdgcn_permlane32_swap(__float_as_uint(sum), __float_as_uint(sum), false, false);
    sum = __uint_as_float(sw[0]) + __uint_as_float(sw[1]);
  }
  float inv = __builtin_amdgcn_rcpf(sum);
  asm volatile("" : "+v"(inv), "+v"(accs[0]), "+v"(accs[1]));
  WAITL0();
  BAR();
  bf16x8 of[16], wf[2][8];
  f32x16 acc[2];
#define PH(s, hf) do { _Pragma("unroll") for (int j = 4 * (hf); j < 4 * (hf) + 4; j += 2) { \
    f32x2 t = {accs[(s) >> 1][8 * ((s) & 1) + j], accs[(s) >> 1][8 * ((s) & 1) + j + 1]}; t *= inv; \
    pf[s][j] = (__bf16)t[0]; pf[s][j + 1] = (__bf16)t[1]; } } while (0)
#define OM(dt, s) acco[dt] = MFMA(vf[dt][s], pf[s], (s) == 0 ? zero16 : acco[dt])
#define OW(i) do { of[i] = pack8(acco[(i) >> 1], (i) & 1); *(LDSP bf16x8*)(smem + ooff + (i) * 1024) = of[i]; } while (0)
  VR(0, 0); VR(1, 0); VR(2, 0); VR(3, 0); VR(0, 1); VR(1, 1); SB0();
  PH(0, 0); PH(0, 1); PH(1, 0); PH(1, 1); PH(2, 0);
  asm volatile("" : "+v"(pf[0]), "+v"(pf[1]));
  SB0();
  OM(0, 0); SB0(); VR(2, 1); VR(3, 1); SB0();
  OM(1, 0); SB0(); VR(0, 2); VR(0, 3); SB0();
  OM(2, 0); SB0(); PH(2, 1); VR(1, 2); SB0();
  OM(3, 0); SB0(); PH(3, 0); VR(1, 3); SB0();
  OM(0, 1); SB0(); PH(3, 1); VR(2, 2); SB0();
  OM(1, 1); SB0(); VR(2, 3); VR(3, 2); SB0();
  OM(2, 1); SB0(); VR(3, 3); SB0(); OM(3, 1); SB0(); OM(0, 2); SB0(); OM(0, 3); SB0(); OM(1, 2); SB0(); OM(1, 3); SB0();
  OW(0); SB0(); OM(2, 2); SB0();
  OW(1); SB0(); OM(2, 3); SB0();
  OW(2); SB0(); OM(3, 2); SB0();
  OW(3); SB0(); OM(3, 3); SB0();
  OW(4); OW(5); SB0(); OW(6); OW(7);
  WAITL0();
  BAR();

#define WR(u, i) wf[(u) & 1][i] = tr_frag(wp, (i) * 2048, (i) * 2048 + 512)
#define PM(u, i) acc[((u) >> 1) & 1] = MFMA(wf[(u) & 1][i], of[8 * ((u) & 1) + (i)], (((u) & 1) == 0 && (i) == 0) ? zero16 : acc[((u) >> 1) & 1])
#define PST(c, a) __builtin_amdgcn_raw_buffer_store_b128(__builtin_bit_cast(u32x4, pack8(acc[(c) & 1], a)), prs, pofs + (c) * 512 + (a) * 256, 0, SC1)
  LDSP char* wp = smem + wa[0] + PBLK(0) * 32768;
#pragma unroll
  for (int i = 0; i < 8; ++i) WR(0, i);
  SB0();
#pragma unroll
  for (int u = 0; u < 16; ++u) {
    if ((u & 1) && u < 15) { WAITL0(); BAR(); }
    if (u < 15) { int wo_ = wa[(u + 1) & 1] + PBLK((u + 1) >> 1) * 32768; asm volatile("" : "+v"(wo_)); wp = smem + wo_; }
    const int c = (u >> 1) - 1;
#pragma unroll
    for (int i = 0; i < 8; ++i) {
      PM(u, i); SB0();
      if (u == 0 && i < 4) {
        of[8 + 2 * i] = *(LDSP bf16x8*)(smem + (ooff ^ 8192) + 2 * i * 1024);
        of[9 + 2 * i] = *(LDSP bf16x8*)(smem + (ooff ^ 8192) + (2 * i + 1) * 1024);
      }
      if (u < 15) { if (i < 2) { WR(u + 1, 2 * i); WR(u + 1, 2 * i + 1); } else if (i < 6) WR(u + 1, i + 2); }
      if ((u & 1) == 0 && u > 0) {
        if (i == 4) PST(c, 0);
        if (i == 5) PST(c, 1);
      }
      SB0();
    }
  }
  PST(7, 0); PST(7, 1);
  const unsigned fofs = lane < 4 ? WS_F + ((b * 16 + hp) * 4 + wid) * 256 + lane * 4 : OOB;
  WAITV(0);
  __builtin_amdgcn_raw_buffer_store_b32(an, prs, fofs, 0, SC1);
  __builtin_amdgcn_s_sleep(40);
  const unsigned vofs = lane < 4 ? WS_F + ((b * 16 + 4 * wid + lane) * 4 + (hp >> 2)) * 256 + (hp & 3) * 4 : OOB;
#pragma unroll 1
  for (int spin = 0; spin < SPIN_CAP; ++spin) {
    const unsigned f = ld_sc1(prs, vofs);
    const bool ok = __all(lane >= 4 || f == tag);
    const unsigned a2 = ld_sc1(prs, aofs);
    if (__any(a2 != an)) { an = a2; __builtin_amdgcn_raw_buffer_store_b32(an, prs, fofs, 0, SC1); }
    if (ok) break;
    __builtin_amdgcn_s_sleep(8);
  }
}

extern "C" void kernel_launch(void* const* d_in, const int* in_sizes, int n_in, void* d_out, int out_size, void* d_ws,
                              size_t ws_size, hipStream_t stream) {
  fused_kernel<<<256, 512, 0, stream>>>((const char*)d_in[0], (const char*)d_in[1], (const char*)d_in[2],
                                        (const char*)d_in[3], (const char*)d_in[4], (char*)d_ws, (char*)d_out);
}
```
